```python
import torch

def _softmax(x, axis=-1):
    return torch.softmax(x, dim=axis)
def _gelu(x):
    return torch.nn.functional.gelu(x, approximate="tanh")
def _silu(x):
    return torch.nn.functional.silu(x)
def _sigmoid(x):
    return torch.sigmoid(x)
def _layernorm(x, axis=-1, eps=1e-5):
    m = x.mean(dim=axis, keepdim=True)
    v = x.var(dim=axis, keepdim=True, unbiased=False)
    return (x - m) / torch.sqrt(v + eps)
def _scatter_add(tgt, idx, src, axis=0):
    return tgt.clone().index_add_(axis, idx, src)
def _segment_sum(src, seg, n):
    out = torch.zeros((n,) + src.shape[1:], dtype=src.dtype)
    return out.index_add_(0, seg, src)
def _pad(x, pw):
    flat = tuple(v for pair in reversed(pw) for v in pair)
    return torch.nn.functional.pad(x, flat)

kernel_name = 'interleave_layers__grouped_kv_mqa__sliding_block__prompt'

def setup_inputs():

    _w = (lambda a: (lambda t: t.to(torch.bfloat16) if t.is_floating_point() else t)(torch.as_tensor(a)))
    def _A_setup():
        B, S, D, H = 1, 2048, 576, 32
        Dh = D // H
        return {
            "x": _w((lambda *s: torch.randn(*s, dtype=torch.bfloat16) * 0.05)(B, S, D)),
            "wq": _w((lambda *s: torch.randn(*s, dtype=torch.bfloat16) * 0.05)(D, H * Dh)),
            "wk": _w((lambda *s: torch.randn(*s, dtype=torch.bfloat16) * 0.05)(D, Dh)),
            "wv": _w((lambda *s: torch.randn(*s, dtype=torch.bfloat16) * 0.05)(D, Dh)),
            "wo": _w((lambda *s: torch.randn(*s, dtype=torch.bfloat16) * 0.05)(H * Dh, D)),
        }
    _A_in = _A_setup()

    def _B_setup():
        B, S, D = 1, 2048, 576
        return {
            "x": _w((lambda *s: torch.randn(*s, dtype=torch.bfloat16) * 0.05)(B, S, D)),
            "wqkv": _w((lambda *s: torch.randn(*s, dtype=torch.bfloat16) * 0.05)(D, 3 * D)),
            "wo": _w((lambda *s: torch.randn(*s, dtype=torch.bfloat16) * 0.05)(D, D)),
        }
    _B_in = _B_setup()

    return {
        "A_x": _A_in["x"],
        "A_wq": _A_in["wq"],
        "A_wk": _A_in["wk"],
        "A_wv": _A_in["wv"],
        "A_wo": _A_in["wo"],
        "B_x": _B_in["x"],
        "B_wqkv": _B_in["wqkv"],
        "B_wo": _B_in["wo"],
    }

def reference(A_x, A_wq, A_wk, A_wv, A_wo, B_x, B_wqkv, B_wo):
    def _A_ref(x, wq, wk, wv, wo):
        B, S, D = x.shape
        H = 32
        Dh = D // H
        q = (lambda x, *d: x.permute(*d))((x @ wq).reshape(B, S, H, Dh), 0, 2, 1, 3)
        k = x @ wk
        v = x @ wv
        scores = torch.einsum("bhqd,bkd->bhqk", q, k) * (Dh ** -0.5)
        p = _softmax(scores, axis=-1)
        o = torch.einsum("bhqk,bkd->bhqd", p, v)
        return (lambda x, *d: x.permute(*d))(o, 0, 2, 1, 3).reshape(B, S, H * Dh) @ wo

    def _B_ref(x, wqkv, wo):
        B, S, D = x.shape
        H = 32; Dh = D // H; W = 256
        qkv = (lambda x, *d: x.permute(*d))((x @ wqkv).reshape(B, S, 3, H, Dh), 2, 0, 3, 1, 4)
        q, k, v = qkv[0], qkv[1], qkv[2]
        scores = torch.einsum("bhqd,bhkd->bhqk", q, k) * (Dh ** -0.5)
        idx = torch.arange(S)
        mask = (idx[:, None] - idx[None, :] >= 0) & (idx[:, None] - idx[None, :] < W)
        scores = torch.where(mask[None, None], scores, -1e9)
        p = _softmax(scores, axis=-1)
        o = torch.einsum("bhqk,bhkd->bhqd", p, v)
        return (lambda x, *d: x.permute(*d))(o, 0, 2, 1, 3).reshape(B, S, D) @ wo

    x = A_x
    x = x + _A_ref(_layernorm(x, axis=-1), A_wq, A_wk, A_wv, A_wo)
    x = x + _B_ref(_layernorm(x, axis=-1), B_wqkv, B_wo)
    x = x + _A_ref(_layernorm(x, axis=-1), A_wq, A_wk, A_wv, A_wo)
    x = x + _B_ref(_layernorm(x, axis=-1), B_wqkv, B_wo)
    return x
```

```cpp
#include <hip/hip_runtime.h>
#include <hip/hip_bf16.h>
#include <stdint.h>

typedef short bf16x8 __attribute__((ext_vector_type(8)));
typedef short bf16x4 __attribute__((ext_vector_type(4)));
typedef __bf16 bf2_t __attribute__((ext_vector_type(2)));
typedef float f32x2 __attribute__((ext_vector_type(2)));
typedef float f32x4 __attribute__((ext_vector_type(4)));
typedef float f32x16 __attribute__((ext_vector_type(16)));
typedef unsigned u32x2 __attribute__((ext_vector_type(2)));
typedef unsigned u32x4 __attribute__((ext_vector_type(4)));
#define LAS __attribute__((address_space(3)))
#define DI __device__ __forceinline__
#define MFMA16x16(a, b, c) __builtin_amdgcn_mfma_f32_16x16x32_bf16(a, b, c, 0, 0, 0)
#define MFMA32(a, b, c) __builtin_amdgcn_mfma_f32_32x32x16_bf16(a, b, c, 0, 0, 0)
#define MFMA32K8(a, b, c) __builtin_amdgcn_mfma_f32_32x32x8bf16_1k(a, b, c, 0, 0, 0)
#define SBAR() __builtin_amdgcn_sched_barrier(0)
#define PIN(x) asm volatile("" : "+v"(x))

constexpr int SEQ = 2048, DM = 576;
constexpr int KSTEPS = 18;
constexpr int FRAG = 1024;
constexpr int TILE_BYTES = 4 * KSTEPS * FRAG;
constexpr int REC = 2368;
constexpr int REC_K1 = 1024, REC_V = 1152;
constexpr float QSCALE = 0.23570226039551584f * 1.4426950408889634f;
constexpr float LN_EPS = 1e-5f;
constexpr float L_LIMIT = 1.8446744e19f;

constexpr size_t XT_BYTES = (size_t)SEQ * DM * 2;
constexpr size_t OFF_XT = 0;
constexpr size_t OFF_OT = OFF_XT + 4 * XT_BYTES;
constexpr size_t KVA_BYTES = 64 * REC;
constexpr size_t OFF_KVA = OFF_OT + 4 * XT_BYTES;
constexpr size_t KVB_BYTES = (size_t)32 * 64 * REC;
constexpr size_t OFF_KVB = OFF_KVA + 2 * KVA_BYTES;
constexpr size_t Q0_BYTES = (size_t)SEQ * 32 * 16 * 2;
constexpr size_t Q1_BYTES = (size_t)SEQ * 32 * 4;
constexpr size_t OFF_Q0 = OFF_KVB + 2 * KVB_BYTES;
constexpr size_t OFF_Q1 = OFF_Q0 + 4 * Q0_BYTES;
constexpr size_t WSL5 = (size_t)8 * 5 * KSTEPS * FRAG;
constexpr size_t OFF_WQA = OFF_Q1 + 4 * Q1_BYTES;
constexpr size_t OFF_WKV = OFF_WQA + WSL5;
constexpr size_t OFF_WOA = OFF_WKV + (size_t)4 * KSTEPS * FRAG;
constexpr size_t OFF_WOB = OFF_WOA + WSL5;
constexpr size_t OFF_WB = OFF_WOB + WSL5;
constexpr size_t WS_END = OFF_WB + (size_t)8 * 14 * KSTEPS * FRAG;

struct Params {
  const unsigned short* x0;  const unsigned short* wq; const unsigned short* wk; const unsigned short* wv; const unsigned short* woA;
  const unsigned short* wqkv; const unsigned short* woB;
  char* ws; unsigned short* out;
};

DI unsigned cvtpk(float a, float b) { f32x2 v = {a, b}; bf2_t r = __builtin_convertvector(v, bf2_t); return __builtin_bit_cast(unsigned, r); }
DI float bflo(unsigned u) { return __uint_as_float(u << 16); }
DI float bfhi(unsigned u) { return __uint_as_float(u & 0xffff0000u); }
DI float dot2(unsigned a, unsigned b, float c) { return __builtin_amdgcn_fdot2_f32_bf16(__builtin_bit_cast(bf2_t, a), __builtin_bit_cast(bf2_t, b), c, false); }
DI float xrow16_sum(float x) {
  auto s = __builtin_amdgcn_permlane16_swap(__float_as_uint(x), __float_as_uint(x), false, false);
  x = __uint_as_float(s[0]) + __uint_as_float(s[1]);
  auto t = __builtin_amdgcn_permlane32_swap(__float_as_uint(x), __float_as_uint(x), false, false);
  return __uint_as_float(t[0]) + __uint_as_float(t[1]);
}
DI float xhalf_max(float x) {
  auto t = __builtin_amdgcn_permlane32_swap(__float_as_uint(x), __float_as_uint(x), false, false);
  return fmaxf(__uint_as_float(t[0]), __uint_as_float(t[1]));
}
DI float lo_half(float x) {
  auto t = __builtin_amdgcn_permlane32_swap(__float_as_uint(x), __float_as_uint(x), false, false);
  return __uint_as_float(t[0]);
}
DI void glds16(const void* g, unsigned lds_base) {
  unsigned sv;
  asm volatile("s_mov_b32 %0, m0\n\ts_mov_b32 m0, %2\n\ts_nop 0\n\tglobal_load_lds_dwordx4 %1, off\n\ts_mov_b32 m0, %0"
               : "=&s"(sv) : "v"(g), "s"(lds_base) : "memory");
}
DI void wait_vm0() { asm volatile("s_waitcnt vmcnt(0)" ::: "memory"); }
DI void decode_wg(int b, int& g, int& m) { const int xcd = b & 7, idx = b >> 3; g = xcd * 4 + (idx >> 3); m = idx & 7; }
DI bf16x8 ld_frag_g(const char* p) { return *(const bf16x8*)p; }
DI bf16x8 ld_frag_l(const LAS char* p) { return *(const LAS bf16x8*)p; }
DI u32x4 as_u4(bf16x8 v) { return __builtin_bit_cast(u32x4, v); }
DI bf16x8 as_bf8(u32x4 v) { return __builtin_bit_cast(bf16x8, v); }
DI bf16x8 ones8() { u32x4 o = {0x3f803f80u, 0x3f803f80u, 0x3f803f80u, 0x3f803f80u}; return as_bf8(o); }

constexpr int J_WQA = 8 * 5 * KSTEPS, J_WKV = 4 * KSTEPS, J_WO = 8 * 5 * KSTEPS, J_WB = 8 * 14 * KSTEPS, J_X = 128 * KSTEPS;
constexpr int J0_WKV = J_WQA, J0_WOA = J0_WKV + J_WKV, J0_WOB = J0_WOA + J_WO, J0_WB = J0_WOB + J_WO, J0_X = J0_WB + J_WB, J0_ONES = J0_X + J_X;
constexpr int N_ONES_CHUNKS = (2 * 64 + 2 * 32 * 64) * 4;
constexpr int J_ONES = N_ONES_CHUNKS / 64, J_TOTAL = J0_ONES + J_ONES;

__global__ void __launch_bounds__(256) k_setup(Params p) {
  const int job = blockIdx.x * 4 + (threadIdx.x >> 6);
  if (job >= J_TOTAL) return;
  const int lane = threadIdx.x & 63, f = lane & 15, g4 = lane >> 4;
  if (job >= J0_ONES) {
    const int c = (job - J0_ONES) * 64 + lane;
    const int rec = c >> 2;
    char* base = rec < 128 ? p.ws + OFF_KVA + (size_t)(rec >> 6) * KVA_BYTES + (size_t)(rec & 63) * REC : p.ws + OFF_KVB + (size_t)(rec - 128) * REC;
    u32x4 o = {0x3f803f80u, 0x3f803f80u, 0x3f803f80u, 0x3f803f80u};
    *(u32x4*)(base + REC_V + ((c & 3) * 19 + 18) * 16) = o;
    return;
  }
  if (job >= J0_X) {
    const int j = job - J0_X, tt = j / KSTEPS, ks = j % KSTEPS;
    const u32x4 v = *(const u32x4*)(p.x0 + (size_t)(tt * 16 + f) * DM + ks * 32 + g4 * 8);
    *(u32x4*)(p.ws + OFF_XT + (size_t)j * FRAG + lane * 16) = v;
    return;
  }
  const unsigned short* src; int ld, col, ks; bool valid = true, operm = false; char* dst;
  if (job < J0_WKV) {
    const int j = job, m = j / (5 * KSTEPS), ft = (j / KSTEPS) % 5; ks = j % KSTEPS;
    src = p.wq; ld = DM; dst = p.ws + OFF_WQA + (size_t)j * FRAG;
    if (ft < 4) col = 18 * (4 * m + ft) + f; else { valid = f < 8; col = 18 * (4 * m + (f >> 1)) + 16 + (f & 1); }
  } else if (job < J0_WOA) {
    const int j = job - J0_WKV, t = j / KSTEPS; ks = j % KSTEPS;
    src = (t < 2) ? p.wk : p.wv; ld = 18; dst = p.ws + OFF_WKV + (size_t)j * FRAG;
    if (t & 1) { valid = f < 2; col = 16 + f; } else col = f;
  } else if (job < J0_WB) {
    const bool isB = job >= J0_WOB; const int j = job - (isB ? J0_WOB : J0_WOA), m = j / (5 * KSTEPS), ft = (j / KSTEPS) % 5; ks = j % KSTEPS;
    src = isB ? p.woB : p.woA; ld = DM; dst = p.ws + (isB ? OFF_WOB : OFF_WOA) + (size_t)j * FRAG; operm = true;
    if (ft < 4) col = 72 * m + 16 * ft + f; else { valid = f < 8; col = 72 * m + 64 + f; }
  } else {
    const int j = job - J0_WB, m = j / (14 * KSTEPS), t = (j / KSTEPS) % 14; ks = j % KSTEPS;
    src = p.wqkv; ld = 3 * DM; dst = p.ws + OFF_WB + (size_t)j * FRAG;
    if (t < 4) col = 18 * (4 * m + t) + f;
    else if (t < 8) col = DM + 18 * (4 * m + t - 4) + f;
    else if (t == 8) col = (f < 8 ? 0 : DM) + 18 * (4 * m + ((f & 7) >> 1)) + 16 + (f & 1);
    else if (t < 13) col = 2 * DM + 18 * (4 * m + t - 9) + f;
    else { valid = f < 8; col = 2 * DM + 18 * (4 * m + ((f & 7) >> 1)) + 16 + (f & 1); }
  }
  unsigned short e[8];
#pragma unroll
  for (int j = 0; j < 8; j++) {
    int k;
    if (!operm) k = 32 * ks + 8 * g4 + j;
    else { const int c = 4 * ks + g4; k = c < 64 ? 18 * (c >> 1) + 8 * (j >> 2) + 4 * (c & 1) + (j & 3) : 18 * (4 * (c - 64) + (j >> 1)) + 16 + (j & 1); }
    e[j] = valid ? src[(size_t)k * ld + col] : (unsigned short)0;
  }
  u32x4 v = {e[0] | ((unsigned)e[1] << 16), e[2] | ((unsigned)e[3] << 16), e[4] | ((unsigned)e[5] << 16), e[6] | ((unsigned)e[7] << 16)};
  *(u32x4*)(dst + lane * 16) = v;
}

constexpr int LDS_STATS = TILE_BYTES;
constexpr int GEMM_LDS = TILE_BYTES + 4 * 64 * 2 * 4;

DI void load_tile_lds(const char* src, char* smem, int w, int lane) {
  const unsigned l0 = (unsigned)(uintptr_t)smem;
#pragma unroll
  for (int i = 0; i < 9; i++) {
    const int fr = i * 8 + w;
    glds16(src + fr * FRAG + lane * 16, __builtin_amdgcn_readfirstlane(l0 + fr * FRAG));
  }
}
DI void stats_acc(bf16x8 x, float& s, float& q) {
  const u32x4 u = as_u4(x);
#pragma unroll
  for (int i = 0; i < 4; i++) { s = dot2(u[i], 0x3f803f80u, s); q = dot2(u[i], u[i], q); }
}
DI void finish_stats(float s, float q, float& mean, float& rstd) {
  mean = s * (1.0f / DM);
  const float var = fmaxf(q * (1.0f / DM) - mean * mean, 0.f);
  rstd = __builtin_amdgcn_rsqf(var + LN_EPS);
}

__global__ void __launch_bounds__(512, 2) k_qa(Params p, int layer) {
  extern __shared__ __attribute__((aligned(16))) char smem[];
  int g, m; decode_wg(blockIdx.x, g, m);
  const int lane = threadIdx.x & 63, w = __builtin_amdgcn_readfirstlane(threadIdx.x >> 6);
  const int r16 = lane & 15, g4 = lane >> 4, ft = w & 3, th = w >> 2;
  load_tile_lds(p.ws + OFF_XT + (size_t)(2 * layer) * XT_BYTES + (size_t)g * TILE_BYTES, smem, w, lane);
  bf16x8 W[KSTEPS], WX[KSTEPS];
  { const char* wp = p.ws + OFF_WQA + (size_t)((m * 5 + ft) * KSTEPS) * FRAG + lane * 16;
#pragma unroll
    for (int ks = 0; ks < KSTEPS; ks++) W[ks] = ld_frag_g(wp + ks * FRAG); }
  const int tkv = m >> 1;
  int exk = 0, te = 0;
  if (w == 0 || w == 1) { exk = 1; te = w; } else if (w == 6 || w == 7) { exk = 1; te = w - 4; }
  else if (th == (tkv >> 1)) { exk = (w & 1) ? 3 : 2; te = tkv; }
  const bool exT = exk >= 2 && (m & 1);
  if (exk) {
    const char* wp = exk == 1 ? p.ws + OFF_WQA + (size_t)((m * 5 + 4) * KSTEPS) * FRAG : p.ws + OFF_WKV + (size_t)(((m & 1) * 2 + (exk - 2)) * KSTEPS) * FRAG;
    wp += lane * 16;
#pragma unroll
    for (int ks = 0; ks < KSTEPS; ks++) WX[ks] = ld_frag_g(wp + ks * FRAG);
  }
  f32x4 cs = {}, csx = {};
  const bf16x8 one = ones8();
#pragma unroll
  for (int ks = 0; ks < KSTEPS; ks++) cs = MFMA16x16(W[ks], one, cs);
  if (exk) {
    if (exT) {
#pragma unroll
      for (int ks = 0; ks < KSTEPS; ks++) csx = MFMA16x16(one, WX[ks], csx);
    } else {
#pragma unroll
      for (int ks = 0; ks < KSTEPS; ks++) csx = MFMA16x16(WX[ks], one, csx);
    }
  }
  wait_vm0();
  __syncthreads();
  const LAS char* A = (const LAS char*)smem + lane * 16;
  f32x4 accx = {};
  if (exk) {
    if (exT) {
#pragma unroll
      for (int ks = 0; ks < KSTEPS; ks++) accx = MFMA16x16(ld_frag_l(A + (te * KSTEPS + ks) * FRAG), WX[ks], accx);
    } else {
#pragma unroll
      for (int ks = 0; ks < KSTEPS; ks++) accx = MFMA16x16(WX[ks], ld_frag_l(A + (te * KSTEPS + ks) * FRAG), accx);
    }
  }
  f32x4 acc0 = {}, acc1 = {};
  float s0 = 0, q0 = 0, s1 = 0, q1 = 0;
#pragma unroll
  for (int ks = 0; ks < KSTEPS; ks++) {
    const bf16x8 x0 = ld_frag_l(A + ((2 * th) * KSTEPS + ks) * FRAG), x1 = ld_frag_l(A + ((2 * th + 1) * KSTEPS + ks) * FRAG);
    acc0 = MFMA16x16(W[ks], x0, acc0);
    acc1 = MFMA16x16(W[ks], x1, acc1);
    if ((ks & 3) == ft) { stats_acc(x0, s0, q0); stats_acc(x1, s1, q1); }
  }
  s0 = xrow16_sum(s0); q0 = xrow16_sum(q0); s1 = xrow16_sum(s1); q1 = xrow16_sum(q1);
  float* st = (float*)(smem + LDS_STATS);
  if (g4 == 0) {
    *(f32x2*)(st + (ft * 64 + (2 * th) * 16 + r16) * 2) = f32x2{s0, q0};
    *(f32x2*)(st + (ft * 64 + (2 * th + 1) * 16 + r16) * 2) = f32x2{s1, q1};
  }
  __syncthreads();
  auto tok_stats = [&](int tok, float& mean, float& rstd) {
    float s = 0, q = 0;
#pragma unroll
    for (int k = 0; k < 4; k++) { const f32x2 v = *(const f32x2*)(st + (k * 64 + tok) * 2); s += v[0]; q += v[1]; }
    finish_stats(s, q, mean, rstd);
  };
  char* Q0 = p.ws + OFF_Q0 + (size_t)(2 * layer) * Q0_BYTES;
  char* Q1 = p.ws + OFF_Q1 + (size_t)(2 * layer) * Q1_BYTES;
  const int qbase = (g * 8 + m) * 4;
  { float m0, r0, m1, r1;
    tok_stats((2 * th) * 16 + r16, m0, r0); tok_stats((2 * th + 1) * 16 + r16, m1, r1);
    r0 *= QSCALE; r1 *= QSCALE;
    const unsigned a0 = cvtpk((acc0[0] - m0 * cs[0]) * r0, (acc0[1] - m0 * cs[1]) * r0), a1 = cvtpk((acc0[2] - m0 * cs[2]) * r0, (acc0[3] - m0 * cs[3]) * r0);
    const unsigned b0 = cvtpk((acc1[0] - m1 * cs[0]) * r1, (acc1[1] - m1 * cs[1]) * r1), b1 = cvtpk((acc1[2] - m1 * cs[2]) * r1, (acc1[3] - m1 * cs[3]) * r1);
    const auto x = __builtin_amdgcn_permlane16_swap(a0, b0, false, false), y = __builtin_amdgcn_permlane16_swap(a1, b1, false, false);
    const u32x4 v = {x[0], y[0], x[1], y[1]};
    const int tok = 16 * (2 * th + (g4 & 1)) + r16;
    *(u32x4*)(Q0 + ((size_t)(qbase + ft) * 64 + tok) * 32 + (g4 >> 1) * 16) = v;
  }
  if (exk) {
    char* KV = p.ws + OFF_KVA + (size_t)layer * KVA_BYTES;
    const int T = (4 * g + te) >> 1, sh = te & 1;
    char* rec = KV + (size_t)T * REC;
    if (!exT) {
      float mean, rstd; tok_stats(16 * te + r16, mean, rstd);
      if (exk == 1) rstd *= QSCALE;
      const unsigned a0 = cvtpk((accx[0] - mean * csx[0]) * rstd, (accx[1] - mean * csx[1]) * rstd), a1 = cvtpk((accx[2] - mean * csx[2]) * rstd, (accx[3] - mean * csx[3]) * rstd);
      if (exk == 1) {
        if (g4 < 2) {
          const int tok = 16 * te + r16;
          *(unsigned*)(Q1 + ((size_t)(qbase + 2 * g4) * 64 + tok) * 4) = a0;
          *(unsigned*)(Q1 + ((size_t)(qbase + 2 * g4 + 1) * 64 + tok) * 4) = a1;
        }
      } else if (exk == 2) {
        *(u32x2*)(rec + ((g4 >> 1) * 32 + 16 * sh + r16) * 16 + (g4 & 1) * 8) = u32x2{a0, a1};
      } else if (g4 == 0) {
        *(unsigned*)(rec + REC_K1 + (16 * sh + r16) * 4) = a0;
      }
    } else {
      float v[4];
#pragma unroll
      for (int i = 0; i < 4; i++) { float mean, rstd; tok_stats(16 * te + 4 * g4 + i, mean, rstd); v[i] = (accx[i] - mean * csx[i]) * rstd; }
      const u32x2 pk = {cvtpk(v[0], v[1]), cvtpk(v[2], v[3])};
      const int d = (exk == 2) ? r16 : 16 + r16;
      if (exk == 2 || r16 < 2) *(u32x2*)(rec + REC_V + ((sh * 2 + (g4 & 1)) * 19 + d) * 16 + (g4 >> 1) * 8) = pk;
    }
  }
}

DI int v_row(int r) { return r < 19 ? r : ((r >= 20 && r < 28) ? 0 : 4); }
DI float tile_max(const f32x16& s) {
  float a = fmaxf(fmaxf(s[0], s[1]), s[2]), b = fmaxf(fmaxf(s[3], s[4]), s[5]);
#pragma unroll
  for (int i = 6; i < 16; i += 2) { a = fmaxf(a, s[i]); b = fmaxf(b, s[i + 1]); }
  return xhalf_max(fmaxf(a, b));
}
DI bf16x4 mk_q1(unsigned tail, float negm, int h) {
  const unsigned mb = cvtpk(negm, 0.f);
  u32x2 t = {h ? 0u : tail, h ? 0u : mb};
  return __builtin_bit_cast(bf16x4, t);
}
DI bf16x4 mk_k1(unsigned tail) { u32x2 t = {tail, 0x00003f80u}; return __builtin_bit_cast(bf16x4, t); }
DI float bf_round(float x) { return bflo(cvtpk(x, 0.f)); }
#define EX(x) __builtin_amdgcn_exp2f(x)
#define X4(S, o) do { S[o] = EX(S[o]); S[o + 1] = EX(S[o + 1]); S[o + 2] = EX(S[o + 2]); S[o + 3] = EX(S[o + 3]); } while (0)
#define C2(S, o, P, pi) do { P[pi] = cvtpk(S[o], S[o + 1]); P[pi + 1] = cvtpk(S[o + 2], S[o + 3]); } while (0)

DI void store_o_tile(char* OT, const f32x16& o, int tok0, int head, int m, int lane) {
  const int r = lane & 31, h = lane >> 5;
  const float inv = __builtin_amdgcn_rcpf(lo_half(o[10]));
  const int tok = tok0 + r, tt = tok >> 4, rr = tok & 15;
  u32x4 v = {cvtpk(o[0] * inv, o[1] * inv), cvtpk(o[2] * inv, o[3] * inv), cvtpk(o[4] * inv, o[5] * inv), cvtpk(o[6] * inv, o[7] * inv)};
  *(u32x4*)(OT + ((size_t)(tt * 72 + 2 * head + h) * 16 + rr) * 16) = v;
  if (h == 0) *(unsigned*)(OT + ((size_t)(tt * 72 + 64 + m) * 16 + rr) * 16 + (head & 3) * 4) = cvtpk(o[8] * inv, o[9] * inv);
}

constexpr int ATTA_LDS = 64 * REC + 4096;
__global__ void __launch_bounds__(512, 2) k_attn_a(Params p, int layer) {
  extern __shared__ __attribute__((aligned(16))) char smem[];
  int g, m; decode_wg(blockIdx.x, g, m);
  const int lane = threadIdx.x & 63, w = __builtin_amdgcn_readfirstlane(threadIdx.x >> 6);
  const int r = lane & 31, h = lane >> 5, hl = w & 3, kh = w >> 2;
  const char* KV = p.ws + OFF_KVA + (size_t)layer * KVA_BYTES;
  const unsigned lds0 = (unsigned)(uintptr_t)smem;
  for (int i = w; i < 148; i += 8) glds16(KV + i * 1024 + lane * 16, __builtin_amdgcn_readfirstlane(lds0 + i * 1024));
  const char* Q0 = p.ws + OFF_Q0 + (size_t)(2 * layer) * Q0_BYTES;
  const char* Q1 = p.ws + OFF_Q1 + (size_t)(2 * layer) * Q1_BYTES;
  const size_t qb = (size_t)((g * 8 + m) * 4 + hl) * 64;
  bf16x8 q0[2]; unsigned qt[2];
#pragma unroll
  for (int a = 0; a < 2; a++) {
    q0[a] = *(const bf16x8*)(Q0 + (qb + 32 * a + r) * 32 + h * 16);
    qt[a] = *(const unsigned*)(Q1 + (qb + 32 * a + r) * 4);
  }
  wait_vm0();
  __syncthreads();
  const f32x16 zero = {};
  const LAS char* l0 = (const LAS char*)smem;
  const int ok0 = lane * 16, ok1 = REC_K1 + r * 4, ov = REC_V + (h * 19 + v_row(r)) * 16;
  float mref[2];
#pragma unroll
  for (int a = 0; a < 2; a++) {
    f32x16 s = MFMA32K8(mk_k1(*(const LAS unsigned*)(l0 + ok1)), mk_q1(qt[a], 0.f, h), zero);
    s = MFMA32(ld_frag_l(l0 + ok0), q0[a], s);
    mref[a] = bf_round(tile_max(s));
  }
  f32x16 oa, ob;
  for (int attempt = 0;; attempt++) {
    const bf16x4 q1a = mk_q1(qt[0], -mref[0], h), q1b = mk_q1(qt[1], -mref[1], h);
    const LAS char* pk0 = l0 + kh * 32 * REC + ok0;
    const LAS char* pk1 = l0 + kh * 32 * REC + ok1;
    const LAS char* pv = l0 + kh * 32 * REC + ov;
    auto ldk0 = [&](int T) { return ld_frag_l(pk0 + T * REC); };
    auto ldk1 = [&](int T) { return mk_k1(*(const LAS unsigned*)(pk1 + T * REC)); };
    auto ldv = [&](int T, int s) { return ld_frag_l(pv + T * REC + s * 608); };
    oa = zero; ob = zero;
    bf16x8 k0A = ldk0(0); bf16x4 k1A = ldk1(0);
    bf16x8 k0B, v0A = ldv(0, 0), v1A = ldv(0, 1), v0B = {}, v1B = {}; bf16x4 k1B;
    f32x16 sa = MFMA32K8(k1A, q1a, zero); sa = MFMA32(k0A, q0[0], sa);
    f32x16 sb = {};
    u32x4 pa0 = {}, pa1 = {}, pb0 = {}, pb1 = {};
#define STEP(K0C, K1C, K0N, K1N, V0C, V1C, V0P, V1P, TN) do { \
      SBAR(); \
      C2(sb, 12, pb1, 2); PIN(pb1); SBAR(); sb = MFMA32K8(K1C, q1b, zero);      X4(sa, 0);  PIN(sa); SBAR(); \
      sb = MFMA32(K0C, q0[1], sb);  K0N = ldk0(TN); K1N = ldk1(TN);             X4(sa, 4);  C2(sa, 0, pa0, 0); PIN(pa0); PIN(sa); SBAR(); \
      ob = MFMA32(V0P, as_bf8(pb0), ob);                                        X4(sa, 8);  C2(sa, 4, pa0, 2); PIN(pa0); PIN(sa); SBAR(); \
      ob = MFMA32(V1P, as_bf8(pb1), ob);                                        X4(sa, 12); C2(sa, 8, pa1, 0); PIN(pa1); PIN(sa); SBAR(); \
      C2(sa, 12, pa1, 2); PIN(pa1); SBAR(); sa = MFMA32K8(K1N, q1a, zero);  V0P = ldv(TN, 0); V1P = ldv(TN, 1); X4(sb, 0);  PIN(sb); SBAR(); \
      sa = MFMA32(K0N, q0[0], sa);                                              X4(sb, 4);  C2(sb, 0, pb0, 0); PIN(pb0); PIN(sb); SBAR(); \
      oa = MFMA32(V0C, as_bf8(pa0), oa);                                        X4(sb, 8);  C2(sb, 4, pb0, 2); PIN(pb0); PIN(sb); SBAR(); \
      oa = MFMA32(V1C, as_bf8(pa1), oa);                                        X4(sb, 12); C2(sb, 8, pb1, 0); PIN(pb1); PIN(sb); SBAR(); \
    } while (0)
    for (int T = 0; T < 32; T += 4) {
      STEP(k0A, k1A, k0B, k1B, v0A, v1A, v0B, v1B, 1);
      STEP(k0B, k1B, k0A, k1A, v0B, v1B, v0A, v1A, 2);
      STEP(k0A, k1A, k0B, k1B, v0A, v1A, v0B, v1B, 3);
      STEP(k0B, k1B, k0A, k1A, v0B, v1B, v0A, v1A, 4);
      pk0 += 4 * REC; pk1 += 4 * REC; pv += 4 * REC;
    }
#undef STEP
    C2(sb, 12, pb1, 2);
    ob = MFMA32(v0B, as_bf8(pb0), ob);
    ob = MFMA32(v1B, as_bf8(pb1), ob);
    const bool bad = (h == 0) && (!(oa[10] < L_LIMIT) || !(ob[10] < L_LIMIT));
    if (attempt || !__any(bad)) break;
    float ma = -3.0e38f, mb = -3.0e38f;
    const bf16x4 z1a = mk_q1(qt[0], 0.f, h), z1b = mk_q1(qt[1], 0.f, h);
    for (int T = 0; T < 32; T++) {
      const LAS char* rp = l0 + (kh * 32 + T) * REC;
      const bf16x8 k0 = ld_frag_l(rp + ok0); const bf16x4 k1 = mk_k1(*(const LAS unsigned*)(rp + ok1));
      f32x16 s = MFMA32K8(k1, z1a, zero); s = MFMA32(k0, q0[0], s); ma = fmaxf(ma, tile_max(s));
      s = MFMA32K8(k1, z1b, zero); s = MFMA32(k0, q0[1], s); mb = fmaxf(mb, tile_max(s));
    }
    mref[0] = bf_round(ma); mref[1] = bf_round(mb);
  }
  __syncthreads();
  float* xch = (float*)smem;
  { const f32x16& og = kh ? oa : ob; const float mg = kh ? mref[0] : mref[1];
    float* d = xch + w * 13 * 64 + lane;
#pragma unroll
    for (int i = 0; i < 12; i++) d[i * 64] = og[i];
    d[12 * 64] = mg; }
  __syncthreads();
  { f32x16 o = kh ? ob : oa; const float mo = kh ? mref[1] : mref[0];
    const float* s = xch + (w ^ 4) * 13 * 64 + lane;
    const float mp = s[12 * 64], mx = fmaxf(mo, mp), fo = EX(mo - mx), fp = EX(mp - mx);
#pragma unroll
    for (int i = 0; i < 12; i++) o[i] = o[i] * fo + s[i * 64] * fp;
    store_o_tile(p.ws + OFF_OT + (size_t)(2 * layer) * XT_BYTES, o, 64 * g + 32 * kh, 4 * m + hl, m, lane);
  }
}

__global__ void __launch_bounds__(512, 2) k_out(Params p, int li) {
  extern __shared__ __attribute__((aligned(16))) char smem[];
  int g, m; decode_wg(blockIdx.x, g, m);
  const int lane = threadIdx.x & 63, w = __builtin_amdgcn_readfirstlane(threadIdx.x >> 6);
  const int r16 = lane & 15, g4 = lane >> 4, ft = w & 3, th = w >> 2;
  load_tile_lds(p.ws + OFF_OT + (size_t)li * XT_BYTES + (size_t)g * TILE_BYTES, smem, w, lane);
  const char* WO = p.ws + ((li & 1) ? OFF_WOB : OFF_WOA);
  bf16x8 W[KSTEPS], WX[KSTEPS];
  { const char* wp = WO + (size_t)((m * 5 + ft) * KSTEPS) * FRAG + lane * 16;
#pragma unroll
    for (int ks = 0; ks < KSTEPS; ks++) W[ks] = ld_frag_g(wp + ks * FRAG); }
  const bool ex = (w < 2) || (w >= 6);
  const int te = w < 2 ? w : w - 4;
  if (ex) {
    const char* wp = WO + (size_t)((m * 5 + 4) * KSTEPS) * FRAG + lane * 16;
#pragma unroll
    for (int ks = 0; ks < KSTEPS; ks++) WX[ks] = ld_frag_g(wp + ks * FRAG);
  }
  const char* XO = p.ws + OFF_XT + (size_t)li * XT_BYTES;
  const int tt0 = 4 * g + 2 * th;
  const int cmain = 9 * m + 2 * ft + (g4 >> 1);
  const u32x2 xo0 = *(const u32x2*)(XO + ((size_t)(tt0 * 72 + cmain) * 16 + r16) * 16 + (g4 & 1) * 8);
  const u32x2 xo1 = *(const u32x2*)(XO + ((size_t)((tt0 + 1) * 72 + cmain) * 16 + r16) * 16 + (g4 & 1) * 8);
  u32x2 xoe = {};
  if (ex) xoe = *(const u32x2*)(XO + ((size_t)((4 * g + te) * 72 + 9 * m + 8) * 16 + r16) * 16 + (g4 & 1) * 8);
  wait_vm0();
  __syncthreads();
  const LAS char* A = (const LAS char*)smem + lane * 16;
  f32x4 accx = {}, acc0 = {}, acc1 = {};
  if (ex) {
#pragma unroll
    for (int ks = 0; ks < KSTEPS; ks++) accx = MFMA16x16(WX[ks], ld_frag_l(A + (te * KSTEPS + ks) * FRAG), accx);
  }
#pragma unroll
  for (int ks = 0; ks < KSTEPS; ks++) {
    acc0 = MFMA16x16(W[ks], ld_frag_l(A + ((2 * th) * KSTEPS + ks) * FRAG), acc0);
    acc1 = MFMA16x16(W[ks], ld_frag_l(A + ((2 * th + 1) * KSTEPS + ks) * FRAG), acc1);
  }
  const unsigned a0 = cvtpk(acc0[0] + bflo(xo0[0]), acc0[1] + bfhi(xo0[0])), a1 = cvtpk(acc0[2] + bflo(xo0[1]), acc0[3] + bfhi(xo0[1]));
  const unsigned b0 = cvtpk(acc1[0] + bflo(xo1[0]), acc1[1] + bfhi(xo1[0])), b1 = cvtpk(acc1[2] + bflo(xo1[1]), acc1[3] + bfhi(xo1[1]));
  const auto x = __builtin_amdgcn_permlane16_swap(a0, b0, false, false), y = __builtin_amdgcn_permlane16_swap(a1, b1, false, false);
  const u32x4 v = {x[0], y[0], x[1], y[1]};
  const unsigned e0 = cvtpk(accx[0] + bflo(xoe[0]), accx[1] + bfhi(xoe[0])), e1 = cvtpk(accx[2] + bflo(xoe[1]), accx[3] + bfhi(xoe[1]));
  if (li < 3) {
    char* XN = p.ws + OFF_XT + (size_t)(li + 1) * XT_BYTES;
    *(u32x4*)(XN + ((size_t)((tt0 + (g4 & 1)) * 72 + cmain) * 16 + r16) * 16) = v;
    if (ex && g4 < 2) *(u32x2*)(XN + ((size_t)((4 * g + te) * 72 + 9 * m + 8) * 16 + r16) * 16 + g4 * 8) = u32x2{e0, e1};
  } else {
    *(u32x4*)(p.out + (size_t)((tt0 + (g4 & 1)) * 16 + r16) * DM + 72 * m + 16 * ft + 8 * (g4 >> 1)) = v;
    if (ex && g4 < 2) *(u32x2*)(p.out + (size_t)((4 * g + te) * 16 + r16) * DM + 72 * m + 64 + 4 * g4) = u32x2{e0, e1};
  }
}

__global__ void __launch_bounds__(512, 2) k_qb(Params p, int layer) {
  extern __shared__ __attribute__((aligned(16))) char smem[];
  int g, m; decode_wg(blockIdx.x, g, m);
  const int lane = threadIdx.x & 63, w = __builtin_amdgcn_readfirstlane(threadIdx.x >> 6);
  const int r16 = lane & 15, g4 = lane >> 4;
  load_tile_lds(p.ws + OFF_XT + (size_t)(2 * layer + 1) * XT_BYTES + (size_t)g * TILE_BYTES, smem, w, lane);
  const int t0 = w < 4 ? 2 * w : (w < 6 ? 9 + 2 * (w - 4) : (w == 6 ? 8 : 13));
  const bool two = w < 6, isT = (w == 4 || w == 5 || w == 7);
  bf16x8 W0[KSTEPS], W1[KSTEPS];
  { const char* wp = p.ws + OFF_WB + (size_t)((m * 14 + t0) * KSTEPS) * FRAG + lane * 16;
#pragma unroll
    for (int ks = 0; ks < KSTEPS; ks++) W0[ks] = ld_frag_g(wp + ks * FRAG);
    if (two) {
#pragma unroll
      for (int ks = 0; ks < KSTEPS; ks++) W1[ks] = ld_frag_g(wp + (KSTEPS + ks) * FRAG);
    } else {
#pragma unroll
      for (int ks = 0; ks < KSTEPS; ks++) W1[ks] = bf16x8{};
    } }
  f32x4 cs0 = {}, cs1 = {};
  const bf16x8 one = ones8();
  if (isT) {
#pragma unroll
    for (int ks = 0; ks < KSTEPS; ks++) { cs0 = MFMA16x16(one, W0[ks], cs0); cs1 = MFMA16x16(one, W1[ks], cs1); }
  } else {
#pragma unroll
    for (int ks = 0; ks < KSTEPS; ks++) { cs0 = MFMA16x16(W0[ks], one, cs0); cs1 = MFMA16x16(W1[ks], one, cs1); }
  }
  wait_vm0();
  __syncthreads();
  const LAS char* A = (const LAS char*)smem + lane * 16;
  f32x4 acc[2][4] = {};
  float ss = 0, qq = 0;
  const int st_t = w & 3, st_p = w >> 2;
#define QB_LOOP(MM) \
  _Pragma("unroll") for (int ks = 0; ks < KSTEPS; ks++) { \
    bf16x8 x[4]; \
    _Pragma("unroll") for (int t = 0; t < 4; t++) x[t] = ld_frag_l(A + (t * KSTEPS + ks) * FRAG); \
    _Pragma("unroll") for (int t = 0; t < 4; t++) acc[0][t] = MM(W0[ks], x[t], acc[0][t]); \
    if (two) { _Pragma("unroll") for (int t = 0; t < 4; t++) acc[1][t] = MM(W1[ks], x[t], acc[1][t]); } \
    if ((ks & 1) == st_p) stats_acc(ld_frag_l(A + (st_t * KSTEPS + ks) * FRAG), ss, qq); \
  }
#define MM_F(wf, xf, c) MFMA16x16(wf, xf, c)
#define MM_T(wf, xf, c) MFMA16x16(xf, wf, c)
  if (isT) { QB_LOOP(MM_T) } else { QB_LOOP(MM_F) }
#undef QB_LOOP
  ss = xrow16_sum(ss); qq = xrow16_sum(qq);
  float* st = (float*)(smem + LDS_STATS);
  if (g4 == 0) *(f32x2*)(st + (st_p * 64 + st_t * 16 + r16) * 2) = f32x2{ss, qq};
  __syncthreads();
  auto tok_stats = [&](int tok, float& mean, float& rstd) {
    const f32x2 a = *(const f32x2*)(st + tok * 2), b = *(const f32x2*)(st + (64 + tok) * 2);
    finish_stats(a[0] + b[0], a[1] + b[1], mean, rstd);
  };
  const int li = 2 * layer + 1;
  char* Q0 = p.ws + OFF_Q0 + (size_t)li * Q0_BYTES;
  char* Q1 = p.ws + OFF_Q1 + (size_t)li * Q1_BYTES;
  char* KV = p.ws + OFF_KVB + (size_t)layer * KVB_BYTES;
  const int qbase = (g * 8 + m) * 4;
  if (!isT) {
    float mean[4], rstd[4];
#pragma unroll
    for (int t = 0; t < 4; t++) tok_stats(16 * t + r16, mean[t], rstd[t]);
    if (two) {
#pragma unroll
      for (int i = 0; i < 2; i++) {
        const int tile = t0 + i; const bool isq = tile < 4; const int hl = tile & 3;
        const f32x4 cs = i ? cs1 : cs0; const float sc = isq ? QSCALE : 1.0f;
#pragma unroll
        for (int u = 0; u < 2; u++) {
          const f32x4 c0 = acc[i][2 * u], c1 = acc[i][2 * u + 1];
          const float m0 = mean[2 * u], m1 = mean[2 * u + 1], r0 = rstd[2 * u] * sc, r1 = rstd[2 * u + 1] * sc;
          const unsigned a0 = cvtpk((c0[0] - m0 * cs[0]) * r0, (c0[1] - m0 * cs[1]) * r0), a1 = cvtpk((c0[2] - m0 * cs[2]) * r0, (c0[3] - m0 * cs[3]) * r0);
          const unsigned b0 = cvtpk((c1[0] - m1 * cs[0]) * r1, (c1[1] - m1 * cs[1]) * r1), b1 = cvtpk((c1[2] - m1 * cs[2]) * r1, (c1[3] - m1 * cs[3]) * r1);
          const auto x = __builtin_amdgcn_permlane16_swap(a0, b0, false, false), y = __builtin_amdgcn_permlane16_swap(a1, b1, false, false);
          const u32x4 v = {x[0], y[0], x[1], y[1]};
          const int tl = 16 * (g4 & 1) + r16;
          if (isq) *(u32x4*)(Q0 + ((size_t)(qbase + hl) * 64 + 32 * u + tl) * 32 + (g4 >> 1) * 16) = v;
          else *(u32x4*)(KV + ((size_t)(4 * m + hl) * 64 + 2 * g + u) * REC + ((g4 >> 1) * 32 + tl) * 16) = v;
        }
      }
    } else {
      const bool isq = g4 < 2; const float sc = isq ? QSCALE : 1.0f; const int hp = 2 * (g4 & 1);
#pragma unroll
      for (int t = 0; t < 4; t++) {
        const f32x4 c = acc[0][t]; const float mm = mean[t], rr = rstd[t] * sc;
        const unsigned a0 = cvtpk((c[0] - mm * cs0[0]) * rr, (c[1] - mm * cs0[1]) * rr), a1 = cvtpk((c[2] - mm * cs0[2]) * rr, (c[3] - mm * cs0[3]) * rr);
        if (isq) {
          *(unsigned*)(Q1 + ((size_t)(qbase + hp) * 64 + 16 * t + r16) * 4) = a0;
          *(unsigned*)(Q1 + ((size_t)(qbase + hp + 1) * 64 + 16 * t + r16) * 4) = a1;
        } else {
          const int T = 2 * g + (t >> 1), key = 16 * (t & 1) + r16;
          *(unsigned*)(KV + ((size_t)(4 * m + hp) * 64 + T) * REC + REC_K1 + key * 4) = a0;
          *(unsigned*)(KV + ((size_t)(4 * m + hp + 1) * 64 + T) * REC + REC_K1 + key * 4) = a1;
        }
      }
    }
  } else {
    const int nt = two ? 2 : 1;
#pragma unroll
    for (int i = 0; i < 2; i++) {
      if (i < nt) {
        const float cs = i ? cs1[0] : cs0[0];
#pragma unroll
        for (int u = 0; u < 2; u++) {
          unsigned pk[2][2];
#pragma unroll
          for (int tt = 0; tt < 2; tt++) {
            float v[4];
#pragma unroll
            for (int k = 0; k < 4; k++) { float mean, rstd; tok_stats(16 * (2 * u + tt) + 4 * g4 + k, mean, rstd); v[k] = (acc[i][2 * u + tt][k] - mean * cs) * rstd; }
            pk[tt][0] = cvtpk(v[0], v[1]); pk[tt][1] = cvtpk(v[2], v[3]);
          }
          const auto x = __builtin_amdgcn_permlane32_swap(pk[0][0], pk[1][0], false, false), y = __builtin_amdgcn_permlane32_swap(pk[0][1], pk[1][1], false, false);
          const u32x4 v = {x[0], y[0], x[1], y[1]};
          if (two) {
            const int hl = t0 - 9 + i;
            *(u32x4*)(KV + ((size_t)(4 * m + hl) * 64 + 2 * g + u) * REC + REC_V + (g4 * 19 + r16) * 16) = v;
          } else if (r16 < 8) {
            *(u32x4*)(KV + ((size_t)(4 * m + (r16 >> 1)) * 64 + 2 * g + u) * REC + REC_V + (g4 * 19 + 16 + (r16 & 1)) * 16) = v;
          }
        }
      }
    }
  }
}

__global__ void __launch_bounds__(512, 2) k_attn_b(Params p, int layer) {
  int g, m; decode_wg(blockIdx.x, g, m);
  const int lane = threadIdx.x & 63, w = __builtin_amdgcn_readfirstlane(threadIdx.x >> 6);
  const int r = lane & 31, h = lane >> 5, hl = w & 3, a = w >> 2;
  const int li = 2 * layer + 1, head = 4 * m + hl, Tq = 2 * g + a;
  const char* KV = p.ws + OFF_KVB + (size_t)layer * KVB_BYTES + (size_t)head * 64 * REC;
  const size_t qi = (size_t)((g * 8 + m) * 4 + hl) * 64 + 32 * a + r;
  const bf16x8 q0 = *(const bf16x8*)(p.ws + OFF_Q0 + (size_t)li * Q0_BYTES + qi * 32 + h * 16);
  const unsigned qt = *(const unsigned*)(p.ws + OFF_Q1 + (size_t)li * Q1_BYTES + qi * 4);
  const int ok0 = lane * 16, ok1 = REC_K1 + r * 4, ov = REC_V + (h * 19 + v_row(r)) * 16;
  const f32x16 zero = {};
  f32x16 mdiag, mfirst;
#pragma unroll
  for (int i = 0; i < 16; i++) { const int c = (i & 3) + 8 * (i >> 2) + 4 * h; mdiag[i] = c <= r ? 0.f : -1e30f; mfirst[i] = c > r ? 0.f : -1e30f; }
  const int Tfirst = Tq - 8, T0 = Tfirst < 0 ? 0 : Tfirst;
  float mref;
  { const char* rp = KV + (size_t)Tq * REC;
    f32x16 s = MFMA32K8(mk_k1(*(const unsigned*)(rp + ok1)), mk_q1(qt, 0.f, h), mdiag);
    s = MFMA32(*(const bf16x8*)(rp + ok0), q0, s);
    mref = bf_round(tile_max(s)); }
  f32x16 o;
  for (int attempt = 0;; attempt++) {
    const bf16x4 q1 = mk_q1(qt, -mref, h);
    o = zero;
    for (int T = T0; T <= Tq; T++) {
      const char* rp = KV + (size_t)T * REC;
      const bf16x8 k0 = *(const bf16x8*)(rp + ok0); const bf16x4 k1 = mk_k1(*(const unsigned*)(rp + ok1));
      const bf16x8 v0 = *(const bf16x8*)(rp + ov), v1 = *(const bf16x8*)(rp + ov + 608);
      f32x16 s;
      if (T == Tq) s = MFMA32K8(k1, q1, mdiag); else if (T == Tfirst) s = MFMA32K8(k1, q1, mfirst); else s = MFMA32K8(k1, q1, zero);
      s = MFMA32(k0, q0, s);
      u32x4 p0, p1;
      X4(s, 0); X4(s, 4); X4(s, 8); X4(s, 12);
      C2(s, 0, p0, 0); C2(s, 4, p0, 2); C2(s, 8, p1, 0); C2(s, 12, p1, 2);
      o = MFMA32(v0, as_bf8(p0), o); o = MFMA32(v1, as_bf8(p1), o);
    }
    const bool bad = (h == 0) && !(o[10] < L_LIMIT);
    if (attempt || !__any(bad)) break;
    float mx = -3.0e38f;
    const bf16x4 z1 = mk_q1(qt, 0.f, h);
    for (int T = T0; T <= Tq; T++) {
      const char* rp = KV + (size_t)T * REC;
      const bf16x4 k1 = mk_k1(*(const unsigned*)(rp + ok1));
      f32x16 s;
      if (T == Tq) s = MFMA32K8(k1, z1, mdiag); else if (T == Tfirst) s = MFMA32K8(k1, z1, mfirst); else s = MFMA32K8(k1, z1, zero);
      s = MFMA32(*(const bf16x8*)(rp + ok0), q0, s);
      mx = fmaxf(mx, tile_max(s));
    }
    mref = bf_round(mx);
  }
  store_o_tile(p.ws + OFF_OT + (size_t)li * XT_BYTES, o, 64 * g + 32 * a, head, m, lane);
}

extern "C" void kernel_launch(void* const* d_in, const int* in_sizes, int n_in, void* d_out, int out_size, void* d_ws, size_t ws_size,
                              hipStream_t stream) {
  Params p;
  p.x0 = (const unsigned short*)d_in[0]; p.wq = (const unsigned short*)d_in[1]; p.wk = (const unsigned short*)d_in[2];
  p.wv = (const unsigned short*)d_in[3]; p.woA = (const unsigned short*)d_in[4];
  p.wqkv = (const unsigned short*)d_in[6]; p.woB = (const unsigned short*)d_in[7];
  p.ws = (char*)d_ws; p.out = (unsigned short*)d_out;
  (void)hipFuncSetAttribute((const void*)k_qa, hipFuncAttributeMaxDynamicSharedMemorySize, GEMM_LDS);
  (void)hipFuncSetAttribute((const void*)k_out, hipFuncAttributeMaxDynamicSharedMemorySize, GEMM_LDS);
  (void)hipFuncSetAttribute((const void*)k_qb, hipFuncAttributeMaxDynamicSharedMemorySize, GEMM_LDS);
  (void)hipFuncSetAttribute((const void*)k_attn_a, hipFuncAttributeMaxDynamicSharedMemorySize, ATTA_LDS);
  k_setup<<<(J_TOTAL + 3) / 4, 256, 0, stream>>>(p);
  for (int l = 0; l < 2; l++) {
    k_qa<<<256, 512, GEMM_LDS, stream>>>(p, l);
    k_attn_a<<<256, 512, ATTA_LDS, stream>>>(p, l);
    k_out<<<256, 512, GEMM_LDS, stream>>>(p, 2 * l);
    k_qb<<<256, 512, GEMM_LDS, stream>>>(p, l);
    k_attn_b<<<256, 512, 0, stream>>>(p, l);
    k_out<<<256, 512, GEMM_LDS, stream>>>(p, 2 * l + 1);
  }
}
```

```cpp
#include <hip/hip_runtime.h>
#include <hip/hip_bf16.h>
#include <stdint.h>

typedef short bf16x8 __attribute__((ext_vector_type(8)));
typedef short bf16x4 __attribute__((ext_vector_type(4)));
typedef __bf16 bf2_t __attribute__((ext_vector_type(2)));
typedef float f32x2 __attribute__((ext_vector_type(2)));
typedef float f32x4 __attribute__((ext_vector_type(4)));
typedef float f32x16 __attribute__((ext_vector_type(16)));
typedef unsigned u32x2 __attribute__((ext_vector_type(2)));
typedef unsigned u32x4 __attribute__((ext_vector_type(4)));
#define LAS __attribute__((address_space(3)))
#define GAS __attribute__((address_space(1)))
#define DI __device__ __forceinline__
#define MFMA16x16(a, b, c) __builtin_amdgcn_mfma_f32_16x16x32_bf16(a, b, c, 0, 0, 0)
#define MFMA32(a, b, c) __builtin_amdgcn_mfma_f32_32x32x16_bf16(a, b, c, 0, 0, 0)
#define MFMA32K8(a, b, c) __builtin_amdgcn_mfma_f32_32x32x8bf16_1k(a, b, c, 0, 0, 0)
#define SBAR() __builtin_amdgcn_sched_barrier(0)
#define PIN(x) asm volatile("" : "+v"(x))

constexpr int SEQ = 2048, DM = 576;
constexpr int KSTEPS = 18;
constexpr int FRAG = 1024;
constexpr int TILE_BYTES = 4 * KSTEPS * FRAG;
constexpr int WSL = 4 * KSTEPS * FRAG + KSTEPS * 512;
constexpr int REC = 2304;
constexpr int REC_K1 = 1024, REC_V = 1152;
constexpr float QSCALE = 0.23570226039551584f * 1.4426950408889634f;
constexpr float LN_EPS = 1e-5f;
constexpr float L_LIMIT = 1.8446744e19f;

constexpr unsigned XT_BYTES = SEQ * DM * 2;
constexpr unsigned OFF_XT = 0;
constexpr unsigned OFF_OT = OFF_XT + 4 * XT_BYTES;
constexpr unsigned KVA_BYTES = 64 * REC;
constexpr unsigned OFF_KVA = OFF_OT + 4 * XT_BYTES;
constexpr unsigned KVB_BYTES = 32 * 64 * REC;
constexpr unsigned OFF_KVB = OFF_KVA + 2 * KVA_BYTES;
constexpr unsigned OFF_WQA = OFF_KVB + 2 * KVB_BYTES;
constexpr unsigned OFF_WOA = OFF_WQA + 8 * WSL;
constexpr unsigned OFF_WOB = OFF_WOA + 8 * WSL;
constexpr unsigned OFF_WKV = OFF_WOB + 8 * WSL;
constexpr unsigned OFF_WB = OFF_WKV + 4 * KSTEPS * FRAG;
constexpr unsigned OFF_CONST = OFF_WB + 8 * 14 * KSTEPS * FRAG;
constexpr unsigned OFF_FLAGS = OFF_CONST + 1024;
constexpr int NFL = 11, F_X = 0, F_O = 3, F_KV = 7, F_KB = 9;
constexpr unsigned FLAG_BYTES = NFL * 32 * 128;
constexpr unsigned WS_END = OFF_FLAGS + FLAG_BYTES;

constexpr int L_A = 0;
constexpr int L_W = TILE_BYTES;
constexpr int L_CONST = KVA_BYTES;
constexpr int L_QST = L_CONST + 1024;
constexpr int L_STATS = L_QST + 9216;
constexpr int LDS_TOTAL = 163840;
static_assert(L_STATS >= L_W + WSL && L_STATS + 2048 <= LDS_TOTAL, "LDS map");

#define STAMP(i) do {} while (0)

struct Params {
  const unsigned short* x0;  const unsigned short* wq; const unsigned short* wk; const unsigned short* wv; const unsigned short* woA;
  const unsigned short* wqkv; const unsigned short* woB;
  char* ws; unsigned short* out;
};

typedef __amdgpu_buffer_rsrc_t rsrc_t;
DI unsigned cvtpk(float a, float b) { f32x2 v = {a, b}; bf2_t r = __builtin_convertvector(v, bf2_t); return __builtin_bit_cast(unsigned, r); }
DI float bflo(unsigned u) { return __uint_as_float(u << 16); }
DI float bfhi(unsigned u) { return __uint_as_float(u & 0xffff0000u); }
DI float dot2(unsigned a, unsigned b, float c) { return __builtin_amdgcn_fdot2_f32_bf16(__builtin_bit_cast(bf2_t, a), __builtin_bit_cast(bf2_t, b), c, false); }
DI float xrow16_sum(float x) {
  auto s = __builtin_amdgcn_permlane16_swap(__float_as_uint(x), __float_as_uint(x), false, false);
  x = __uint_as_float(s[0]) + __uint_as_float(s[1]);
  auto t = __builtin_amdgcn_permlane32_swap(__float_as_uint(x), __float_as_uint(x), false, false);
  return __uint_as_float(t[0]) + __uint_as_float(t[1]);
}
DI float xhalf_max(float x) {
  auto t = __builtin_amdgcn_permlane32_swap(__float_as_uint(x), __float_as_uint(x), false, false);
  return fmaxf(__uint_as_float(t[0]), __uint_as_float(t[1]));
}
DI float lo_half(float x) {
  auto t = __builtin_amdgcn_permlane32_swap(__float_as_uint(x), __float_as_uint(x), false, false);
  return __uint_as_float(t[0]);
}
template <bool SC1> DI void glds16(const char* base, unsigned voff, unsigned lds_base) {
  unsigned sv;
  if (SC1) asm volatile("s_mov_b32 %0, m0\n\ts_mov_b32 m0, %3\n\ts_nop 0\n\tglobal_load_lds_dwordx4 %1, %2 sc1\n\ts_mov_b32 m0, %0"
                        : "=&s"(sv) : "v"(voff), "s"(base), "s"(lds_base) : "memory");
  else     asm volatile("s_mov_b32 %0, m0\n\ts_mov_b32 m0, %3\n\ts_nop 0\n\tglobal_load_lds_dwordx4 %1, %2\n\ts_mov_b32 m0, %0"
                        : "=&s"(sv) : "v"(voff), "s"(base), "s"(lds_base) : "memory");
}
DI void wait_vm0() { asm volatile("s_waitcnt vmcnt(0)" ::: "memory"); }
DI void wg_barrier() { asm volatile("s_waitcnt lgkmcnt(0)\n\ts_barrier" ::: "memory"); }
DI bf16x8 ld_frag_l(const LAS char* p) { return *(const LAS bf16x8*)p; }
DI u32x4 as_u4(bf16x8 v) { return __builtin_bit_cast(u32x4, v); }
DI bf16x8 as_bf8(u32x4 v) { return __builtin_bit_cast(bf16x8, v); }
DI bf16x8 ones8() { u32x4 o = {0x3f803f80u, 0x3f803f80u, 0x3f803f80u, 0x3f803f80u}; return as_bf8(o); }
DI u32x4 ld16(rsrc_t r, unsigned off) { return __builtin_amdgcn_raw_buffer_load_b128(r, off, 0, 0); }
DI u32x4 ld16c(rsrc_t r, unsigned off) { return __builtin_amdgcn_raw_buffer_load_b128(r, off, 0, 16); }
DI u32x2 ld8c(rsrc_t r, unsigned off) { return __builtin_amdgcn_raw_buffer_load_b64(r, off, 0, 16); }
DI unsigned ld4c(rsrc_t r, unsigned off) { return __builtin_amdgcn_raw_buffer_load_b32(r, off, 0, 16); }
DI void st16c(rsrc_t r, unsigned off, u32x4 v) { __builtin_amdgcn_raw_buffer_store_b128(v, r, off, 0, 16); }
DI void st8c(rsrc_t r, unsigned off, u32x2 v) { __builtin_amdgcn_raw_buffer_store_b64(v, r, off, 0, 16); }
DI void st4c(rsrc_t r, unsigned off, unsigned v) { __builtin_amdgcn_raw_buffer_store_b32(v, r, off, 0, 16); }

DI unsigned short lds_u16(const char* smem, int byte) { return *(const LAS unsigned short*)((const LAS char*)smem + byte); }
template <class F> DI u32x4 gather8(F addr, const char* smem) {
  u32x4 v;
#pragma unroll
  for (int i = 0; i < 4; i++) v[i] = lds_u16(smem, addr(2 * i)) | ((unsigned)lds_u16(smem, addr(2 * i + 1)) << 16);
  return v;
}
DI int operm(int ks, int g4, int j) {
  const int c = 4 * ks + g4;
  return c < 64 ? 18 * (c >> 1) + 8 * (j >> 2) + 4 * (c & 1) + (j & 3) : 18 * (4 * (c - 64) + (j >> 1)) + 16 + (j & 1);
}
__global__ void __launch_bounds__(512) k_setup(Params p) {
  __shared__ __attribute__((aligned(16))) char smem[42 * 1024];
  const int job = blockIdx.x, t = threadIdx.x;
  if (job >= 128) {
    const int tt = job - 128;
    for (int c = t; c < 72 * 16; c += 512) {
      const int ch = c >> 4, r = c & 15;
      *(u32x4*)(p.ws + OFF_XT + ((size_t)tt * 72 * 16 + c) * 16) = *(const u32x4*)(p.x0 + (size_t)(tt * 16 + r) * DM + ch * 8);
    }
    return;
  }
  if (job == 127) {
    if (t < 64) { const unsigned v = (t == 11 || t == 13 || t == 47 || t == 49) ? 0x3f803f80u : 0u; *(u32x4*)(p.ws + OFF_CONST + t * 16) = u32x4{v, v, v, v}; }
    for (int i = t; i < (int)(FLAG_BYTES / 16); i += 512) *(u32x4*)(p.ws + OFF_FLAGS + i * 16) = u32x4{0, 0, 0, 0};
    return;
  }
  if (job == 126) {
    for (int c = t; c < 2 * 1296; c += 512) { const int a = c / 1296, i = c % 1296; *(LAS u32x4*)((LAS char*)smem + c * 16) = *(const u32x4*)((a ? p.wv : p.wk) + i * 8); }
    __syncthreads();
    for (int oc = t; oc < 4 * KSTEPS * 64; oc += 512) {
      const int fr = oc >> 6, lane = oc & 63, f = lane & 15, g4 = lane >> 4, tl = fr / KSTEPS, ks = fr % KSTEPS;
      const bool valid = !(tl & 1) || f < 2; const int col = (tl & 1) ? 16 + (f & 1) : f;
      u32x4 v = gather8([&](int j) { return (tl >> 1) * 20736 + ((32 * ks + 8 * g4 + j) * 18 + col) * 2; }, smem);
      if (!valid) v = u32x4{0, 0, 0, 0};
      *(u32x4*)(p.ws + OFF_WKV + (size_t)oc * 16) = v;
    }
    return;
  }
  if (job < 18 || job >= 90) {
    const bool isq = job < 18; const bool isB = job >= 108; const int ks = isq ? job : job - (isB ? 108 : 90);
    const unsigned short* src = isq ? p.wq : (isB ? p.woB : p.woA);
    for (int c = t; c < 32 * 72; c += 512) {
      const int rr = c / 72, ch = c % 72, row = isq ? 32 * ks + rr : operm(ks, rr >> 3, rr & 7);
      *(LAS u32x4*)((LAS char*)smem + c * 16) = *(const u32x4*)(src + (size_t)row * DM + ch * 8);
    }
    __syncthreads();
    char* dst = p.ws + (isq ? OFF_WQA : (isB ? OFF_WOB : OFF_WOA));
    for (int oc = t; oc < 8 * (4 * 64 + 32); oc += 512) {
      const int m = oc / 288, e = oc % 288; int f, g4, col, off;
      if (e < 256) { const int ft = e >> 6, lane = e & 63; f = lane & 15; g4 = lane >> 4; col = isq ? 18 * (4 * m + ft) + f : 72 * m + 16 * ft + f; off = (ft * KSTEPS + ks) * FRAG + lane * 16; }
      else { const int l = e - 256; f = l & 7; g4 = l >> 3; col = isq ? 18 * (4 * m + (f >> 1)) + 16 + (f & 1) : 72 * m + 64 + f; off = 4 * KSTEPS * FRAG + ks * 512 + l * 16; }
      *(u32x4*)(dst + (size_t)m * WSL + off) = gather8([&](int j) { return (8 * g4 + j) * 1152 + col * 2; }, smem);
    }
    return;
  }
  {
    const int j0 = job - 18, ks = j0 >> 2, mp = j0 & 3;
    for (int c = t; c < 32 * 54; c += 512) {
      const int rr = c / 54, e = c % 54, part = e / 18, ch = e % 18;
      *(LAS u32x4*)((LAS char*)smem + c * 16) = *(const u32x4*)(p.wqkv + (size_t)(32 * ks + rr) * (3 * DM) + part * DM + mp * 144 + ch * 8);
    }
    __syncthreads();
    for (int oc = t; oc < 2 * 14 * 64; oc += 512) {
      const int mm = oc / (14 * 64), tl = (oc >> 6) % 14, lane = oc & 63, f = lane & 15, g4 = lane >> 4;
      int part, col; bool valid = true;
      if (tl < 4) { part = 0; col = 72 * mm + 18 * tl + f; }
      else if (tl < 8) { part = 1; col = 72 * mm + 18 * (tl - 4) + f; }
      else if (tl == 8) { part = f >> 3; col = 72 * mm + 18 * ((f & 7) >> 1) + 16 + (f & 1); }
      else if (tl < 13) { part = 2; col = 72 * mm + 18 * (tl - 9) + f; }
      else { part = 2; valid = f < 8; col = 72 * mm + 18 * ((f & 7) >> 1) + 16 + (f & 1); }
      u32x4 v = gather8([&](int j) { return ((8 * g4 + j) * 54 + part * 18) * 16 + col * 2; }, smem);
      if (!valid) v = u32x4{0, 0, 0, 0};
      *(u32x4*)(p.ws + OFF_WB + ((size_t)((2 * mp + mm) * 14 + tl) * KSTEPS + ks) * FRAG + lane * 16) = v;
    }
  }
}

struct Ctx {
  Params p; rsrc_t rs; char* smem; unsigned lds0;
  int g, m, lane, w, r16, g4;
};
DI Ctx fresh(const Ctx& c0) {
  Ctx c = c0;
  asm volatile("" : "+v"(c.lane), "+s"(c.w), "+s"(c.g), "+s"(c.m), "+s"(c.lds0));
  c.r16 = c.lane & 15; c.g4 = c.lane >> 4;
  return c;
}
DI unsigned flag_off(int id, int g, int word) { return OFF_FLAGS + ((id * 32 + g) * 32 + word) * 4; }
DI void flag_set(const Ctx& c, int id, int word) { __hip_atomic_store((GAS unsigned*)(c.p.ws + flag_off(id, c.g, word)), 1u, __ATOMIC_RELAXED, __HIP_MEMORY_SCOPE_AGENT); }
DI void poll(const Ctx& c, unsigned off, bool need) {
  for (unsigned spins = 0; spins < (1u << 17); spins++) {
    const unsigned v = need ? __hip_atomic_load((GAS unsigned*)(c.p.ws + off), __ATOMIC_RELAXED, __HIP_MEMORY_SCOPE_AGENT) : 1u;
    if (__all(v != 0u)) break;
    __builtin_amdgcn_s_sleep(1);
  }
}
DI void publish(const Ctx& c, int id) { wait_vm0(); wg_barrier(); if (threadIdx.x == 0) flag_set(c, id, c.m); }
DI void wait_group(const Ctx& c, int id) { if (c.w == 7) poll(c, flag_off(id, c.g, c.lane & 7), c.lane < 8); wg_barrier(); }

template <bool SC1> DI void dma_tile(const Ctx& c, unsigned src) {
#pragma unroll
  for (int i = 0; i < 9; i++) {
    const int fr = i * 8 + c.w;
    glds16<SC1>(c.p.ws, src + fr * FRAG + c.lane * 16, __builtin_amdgcn_readfirstlane(c.lds0 + L_A + fr * FRAG));
  }
}
DI void dma_wslice(const Ctx& c, unsigned src) {
#pragma unroll
  for (int i = 0; i < 11; i++) {
    const int fr = i * 8 + c.w;
    if (fr < 81) glds16<false>(c.p.ws, src + fr * FRAG + c.lane * 16, __builtin_amdgcn_readfirstlane(c.lds0 + L_W + fr * FRAG));
  }
}
DI void stats_acc(bf16x8 x, float& s, float& q) {
  const u32x4 u = as_u4(x);
#pragma unroll
  for (int i = 0; i < 4; i++) { s = dot2(u[i], 0x3f803f80u, s); q = dot2(u[i], u[i], q); }
}
DI void finish_stats(float s, float q, float& mean, float& rstd) {
  mean = s * (1.0f / DM);
  const float var = fmaxf(q * (1.0f / DM) - mean * mean, 0.f);
  rstd = __builtin_amdgcn_rsqf(var + LN_EPS);
}
DI void wregs_from_lds(const Ctx& c, bf16x8 (&W)[KSTEPS], bf16x8 (&WX)[KSTEPS], bool tail) {
  const LAS char* wl = (const LAS char*)c.smem + L_W;
#pragma unroll
  for (int ks = 0; ks < KSTEPS; ks++) W[ks] = ld_frag_l(wl + ((c.w & 3) * KSTEPS + ks) * FRAG + c.lane * 16);
  if (tail) {
#pragma unroll
    for (int ks = 0; ks < KSTEPS; ks++) WX[ks] = ld_frag_l(wl + 4 * KSTEPS * FRAG + ks * 512 + (c.g4 * 8 + (c.r16 & 7)) * 16);
  }
}

DI float tile_max(const f32x16& s) {
  float a = fmaxf(fmaxf(s[0], s[1]), s[2]), b = fmaxf(fmaxf(s[3], s[4]), s[5]);
#pragma unroll
  for (int i = 6; i < 16; i += 2) { a = fmaxf(a, s[i]); b = fmaxf(b, s[i + 1]); }
  return xhalf_max(fmaxf(a, b));
}
DI bf16x4 mk_q1(unsigned tail, float negm, int h) {
  const unsigned mb = cvtpk(negm, 0.f);
  u32x2 t = {h ? 0u : tail, h ? 0u : mb};
  return __builtin_bit_cast(bf16x4, t);
}
DI bf16x4 mk_k1(unsigned tail) { u32x2 t = {tail, 0x00003f80u}; return __builtin_bit_cast(bf16x4, t); }
DI float bf_round(float x) { return bflo(cvtpk(x, 0.f)); }
DI int v_const_off(int r, int h) { return r == 18 ? (11 + 2 * h) * 16 : ((r >= 20 && r < 28) ? (12 + 2 * h) * 16 : (10 + 2 * h) * 16); }
#define EX(x) __builtin_amdgcn_exp2f(x)
#define X4(S, o) do { S[o] = EX(S[o]); S[o + 1] = EX(S[o + 1]); S[o + 2] = EX(S[o + 2]); S[o + 3] = EX(S[o + 3]); } while (0)
#define C2(S, o, P, pi) do { P[pi] = cvtpk(S[o], S[o + 1]); P[pi + 1] = cvtpk(S[o + 2], S[o + 3]); } while (0)

DI void store_o_tile(const Ctx& c, unsigned OT, const f32x16& o, int tok0, int head) {
  const int r = c.lane & 31, h = c.lane >> 5;
  const float inv = __builtin_amdgcn_rcpf(lo_half(o[10]));
  const int tok = tok0 + r, tt = tok >> 4, rr = tok & 15;
  const u32x4 v = {cvtpk(o[0] * inv, o[1] * inv), cvtpk(o[2] * inv, o[3] * inv), cvtpk(o[4] * inv, o[5] * inv), cvtpk(o[6] * inv, o[7] * inv)};
  st16c(c.rs, OT + ((tt * 72 + 2 * head + h) * 16 + rr) * 16, v);
  if (h == 0) st4c(c.rs, OT + ((tt * 72 + 64 + c.m) * 16 + rr) * 16 + (head & 3) * 4, cvtpk(o[8] * inv, o[9] * inv));
}
DI void q_from_stage(const Ctx& c, int hl, int tok, bf16x8& q0, unsigned& qt) {
  const LAS char* qs = (const LAS char*)c.smem + L_QST;
  q0 = ld_frag_l(qs + (hl * 64 + tok) * 32 + (c.lane >> 5) * 16);
  qt = *(const LAS unsigned*)(qs + 8192 + (hl * 64 + tok) * 4);
}

DI void unit_a(const Ctx& c_in, int l, bool first) {
  const Ctx c = fresh(c_in);
  const Params& p = c.p;
  const int lane = c.lane, w = c.w, r16 = c.r16, g4 = c.g4, g = c.g, m = c.m, ft = w & 3, th = w >> 2;
  char* smem = c.smem;
  if (first) dma_wslice(c, OFF_WQA + m * WSL);
  const int tkv = m >> 1;
  int exk = 0, te = 0;
  if (w == 0 || w == 1) { exk = 1; te = w; } else if (w == 6 || w == 7) { exk = 1; te = w - 4; }
  else if (th == (tkv >> 1)) { exk = (w & 1) ? 3 : 2; te = tkv; }
  const bool exT = exk >= 2 && (m & 1);
  bf16x8 W[KSTEPS], WX[KSTEPS];
  if (exk >= 2) {
    const unsigned wo = OFF_WKV + ((m & 1) * 2 + (exk - 2)) * KSTEPS * FRAG + lane * 16;
#pragma unroll
    for (int ks = 0; ks < KSTEPS; ks++) WX[ks] = as_bf8(ld16(c.rs, wo + ks * FRAG));
  }
  if (l > 0) wait_group(c, F_X + 2 * l - 1);
  if (first) { wait_vm0(); wg_barrier(); }
  dma_tile<true>(c, OFF_XT + 2 * l * XT_BYTES + g * TILE_BYTES);
  wregs_from_lds(c, W, WX, exk == 1);
  f32x4 cs = {}, csx = {};
  const bf16x8 one = ones8();
#pragma unroll
  for (int ks = 0; ks < KSTEPS; ks++) cs = MFMA16x16(W[ks], one, cs);
  if (exk) {
    if (exT) {
#pragma unroll
      for (int ks = 0; ks < KSTEPS; ks++) csx = MFMA16x16(one, WX[ks], csx);
    } else {
#pragma unroll
      for (int ks = 0; ks < KSTEPS; ks++) csx = MFMA16x16(WX[ks], one, csx);
    }
  }
  wait_vm0();
  wg_barrier();
  STAMP(8 * l + 1);
  const LAS char* A = (const LAS char*)smem + L_A + lane * 16;
  float* st = (float*)(smem + L_STATS);
  { float s0 = 0, q0 = 0, s1 = 0, q1 = 0;
#pragma unroll
    for (int i = 0; i < 5; i++) {
      const int ks = 4 * i + ft;
      if (ks < KSTEPS) { stats_acc(ld_frag_l(A + ((2 * th) * KSTEPS + ks) * FRAG), s0, q0); stats_acc(ld_frag_l(A + ((2 * th + 1) * KSTEPS + ks) * FRAG), s1, q1); }
    }
    s0 = xrow16_sum(s0); q0 = xrow16_sum(q0); s1 = xrow16_sum(s1); q1 = xrow16_sum(q1);
    if (g4 == 0) {
      *(f32x2*)(st + (ft * 64 + (2 * th) * 16 + r16) * 2) = f32x2{s0, q0};
      *(f32x2*)(st + (ft * 64 + (2 * th + 1) * 16 + r16) * 2) = f32x2{s1, q1};
    } }
  f32x4 accx = {};
  if (exk) {
    if (exT) {
#pragma unroll
      for (int ks = 0; ks < KSTEPS; ks++) accx = MFMA16x16(ld_frag_l(A + (te * KSTEPS + ks) * FRAG), WX[ks], accx);
    } else {
#pragma unroll
      for (int ks = 0; ks < KSTEPS; ks++) accx = MFMA16x16(WX[ks], ld_frag_l(A + (te * KSTEPS + ks) * FRAG), accx);
    }
  }
  wg_barrier();
  auto tok_stats = [&](int tok, float& mean, float& rstd) {
    float s = 0, q = 0;
#pragma unroll
    for (int k = 0; k < 4; k++) { const f32x2 v = *(const f32x2*)(st + (k * 64 + tok) * 2); s += v[0]; q += v[1]; }
    finish_stats(s, q, mean, rstd);
  };
  LAS char* qs = (LAS char*)smem + L_QST;
  unsigned qtail0 = 0, qtail1 = 0;
  if (exk) {
    const unsigned rec = OFF_KVA + l * KVA_BYTES + ((4 * g + te) >> 1) * REC; const int sh = te & 1;
    if (!exT) {
      float mean, rstd; tok_stats(16 * te + r16, mean, rstd);
      if (exk == 1) rstd *= QSCALE;
      const unsigned a0 = cvtpk((accx[0] - mean * csx[0]) * rstd, (accx[1] - mean * csx[1]) * rstd), a1 = cvtpk((accx[2] - mean * csx[2]) * rstd, (accx[3] - mean * csx[3]) * rstd);
      if (exk == 1) { qtail0 = a0; qtail1 = a1; }
      else if (exk == 2) st8c(c.rs, rec + ((g4 >> 1) * 32 + 16 * sh + r16) * 16 + (g4 & 1) * 8, u32x2{a0, a1});
      else if (g4 == 0) st4c(c.rs, rec + REC_K1 + (16 * sh + r16) * 4, a0);
    } else {
      float v[4];
#pragma unroll
      for (int i = 0; i < 4; i++) { float mean, rstd; tok_stats(16 * te + 4 * g4 + i, mean, rstd); v[i] = (accx[i] - mean * csx[i]) * rstd; }
      const int d = (exk == 2) ? r16 : 16 + r16;
      if (exk == 2 || r16 < 2) st8c(c.rs, rec + REC_V + ((sh * 2 + (g4 & 1)) * 18 + d) * 16 + (g4 >> 1) * 8, u32x2{cvtpk(v[0], v[1]), cvtpk(v[2], v[3])});
    }
    if (exk >= 2) { wait_vm0(); if (lane == 0) flag_set(c, F_KV + l, 2 * m + (exk - 2)); }
  }
  { f32x4 acc0 = {}, acc1 = {};
#pragma unroll
    for (int ks = 0; ks < KSTEPS; ks++) {
      acc0 = MFMA16x16(W[ks], ld_frag_l(A + ((2 * th) * KSTEPS + ks) * FRAG), acc0);
      acc1 = MFMA16x16(W[ks], ld_frag_l(A + ((2 * th + 1) * KSTEPS + ks) * FRAG), acc1);
    }
    float m0, r0, m1, r1;
    tok_stats((2 * th) * 16 + r16, m0, r0); tok_stats((2 * th + 1) * 16 + r16, m1, r1);
    r0 *= QSCALE; r1 *= QSCALE;
    const unsigned a0 = cvtpk((acc0[0] - m0 * cs[0]) * r0, (acc0[1] - m0 * cs[1]) * r0), a1 = cvtpk((acc0[2] - m0 * cs[2]) * r0, (acc0[3] - m0 * cs[3]) * r0);
    const unsigned b0 = cvtpk((acc1[0] - m1 * cs[0]) * r1, (acc1[1] - m1 * cs[1]) * r1), b1 = cvtpk((acc1[2] - m1 * cs[2]) * r1, (acc1[3] - m1 * cs[3]) * r1);
    const auto x = __builtin_amdgcn_permlane16_swap(a0, b0, false, false), y = __builtin_amdgcn_permlane16_swap(a1, b1, false, false);
    *(LAS u32x4*)(qs + (ft * 64 + 16 * (2 * th + (g4 & 1)) + r16) * 32 + (g4 >> 1) * 16) = u32x4{x[0], y[0], x[1], y[1]};
    if (exk == 1 && g4 < 2) {
      *(LAS unsigned*)(qs + 8192 + ((2 * g4) * 64 + 16 * te + r16) * 4) = qtail0;
      *(LAS unsigned*)(qs + 8192 + ((2 * g4 + 1) * 64 + 16 * te + r16) * 4) = qtail1;
    } }
  if (w == 5) { const unsigned v = (lane == 11 || lane == 13 || lane == 47 || lane == 49) ? 0x3f803f80u : 0u; *(LAS u32x4*)((LAS char*)smem + L_CONST + lane * 16) = u32x4{v, v, v, v}; }
  STAMP(8 * l + 2);
  const int r = lane & 31, h = lane >> 5, hl = w & 3, kh = w >> 2;
  const int c0 = (blockIdx.x & 3);
  const unsigned KV = OFF_KVA + l * KVA_BYTES;
  if (w == 0) {
    poll(c, flag_off(F_KV + l, 4 * c0 + (lane >> 4), lane & 15), true);
    poll(c, flag_off(F_KV + l, 16 + 4 * c0 + (lane >> 4), lane & 15), true);
  }
  wg_barrier();
  bf16x8 q0[2]; unsigned qt[2];
#pragma unroll
  for (int a = 0; a < 2; a++) q_from_stage(c, hl, 32 * a + r, q0[a], qt[a]);
#pragma unroll
  for (int i = 0; i < 5; i++) {
    const int pc = i * 8 + w;
    if (pc < 36) { const unsigned o = (pc >= 18 ? 32 * REC : 0) + c0 * 8 * REC + (pc >= 18 ? pc - 18 : pc) * 1024;
      glds16<true>(p.ws, KV + o + lane * 16, __builtin_amdgcn_readfirstlane(c.lds0 + o)); }
  }
  if (w == 0) {
#pragma unroll
    for (int i = 1; i < 4; i++) {
      poll(c, flag_off(F_KV + l, 4 * ((c0 + i) & 3) + (lane >> 4), lane & 15), true);
      poll(c, flag_off(F_KV + l, 16 + 4 * ((c0 + i) & 3) + (lane >> 4), lane & 15), true);
    }
  }
  wait_vm0();
  wg_barrier();
#pragma unroll
  for (int i = 0; i < 14; i++) {
    const int pc = i * 8 + w;
    if (pc < 108) { const int hf = pc >= 54, q = hf ? pc - 54 : pc, ch = (c0 + 1 + q / 18) & 3;
      const unsigned o = hf * 32 * REC + ch * 8 * REC + (q % 18) * 1024;
      glds16<true>(p.ws, KV + o + lane * 16, __builtin_amdgcn_readfirstlane(c.lds0 + o)); }
  }
  STAMP(8 * l + 3);
  const f32x16 zero = {};
  const LAS char* l0 = (const LAS char*)smem;
  const LAS char* hb = l0 + kh * 32 * REC;
  const int ok0 = lane * 16, ok1 = REC_K1 + r * 4;
  const bool vreal = r < 18;
  const int ovn = REC_V + h * 288 + r * 16;
  const LAS char* vconst = l0 + L_CONST + v_const_off(r, h);
  const int vstep = vreal ? REC : 0;
  float mref[2];
#pragma unroll
  for (int a = 0; a < 2; a++) {
    const LAS char* rp = hb + c0 * 8 * REC;
    f32x16 s = MFMA32K8(mk_k1(*(const LAS unsigned*)(rp + ok1)), mk_q1(qt[a], 0.f, h), zero);
    s = MFMA32(ld_frag_l(rp + ok0), q0[a], s);
    mref[a] = bf_round(tile_max(s));
  }
  f32x16 oa, ob;
  for (int attempt = 0;; attempt++) {
    const bf16x4 q1a = mk_q1(qt[0], -mref[0], h), q1b = mk_q1(qt[1], -mref[1], h);
    const LAS char* pk = hb + c0 * 8 * REC;
    const LAS char* pv = vreal ? pk + ovn : vconst;
    oa = zero; ob = zero;
    bf16x8 k0A = ld_frag_l(pk + ok0); bf16x4 k1A = mk_k1(*(const LAS unsigned*)(pk + ok1));
    bf16x8 k0B, v0A = ld_frag_l(pv), v1A = ld_frag_l(pv + 576), v0B = {}, v1B = {}; bf16x4 k1B;
    f32x16 sa = MFMA32K8(k1A, q1a, zero); sa = MFMA32(k0A, q0[0], sa);
    f32x16 sb = {};
    u32x4 pa0 = {}, pa1 = {}, pb0 = {}, pb1 = {};
#define STEP(K0C, K1C, K0N, K1N, V0C, V1C, V0P, V1P, PN, PVN) do { \
      SBAR(); \
      C2(sb, 12, pb1, 2); PIN(pb1); SBAR(); sb = MFMA32K8(K1C, q1b, zero);      X4(sa, 0);  PIN(sa); SBAR(); \
      sb = MFMA32(K0C, q0[1], sb);  K0N = ld_frag_l((PN) + ok0); K1N = mk_k1(*(const LAS unsigned*)((PN) + ok1)); X4(sa, 4);  C2(sa, 0, pa0, 0); PIN(pa0); PIN(sa); SBAR(); \
      ob = MFMA32(V0P, as_bf8(pb0), ob);                                        X4(sa, 8);  C2(sa, 4, pa0, 2); PIN(pa0); PIN(sa); SBAR(); \
      ob = MFMA32(V1P, as_bf8(pb1), ob);                                        X4(sa, 12); C2(sa, 8, pa1, 0); PIN(pa1); PIN(sa); SBAR(); \
      C2(sa, 12, pa1, 2); PIN(pa1); SBAR(); sa = MFMA32K8(K1N, q1a, zero);  pv = (PVN); V0P = ld_frag_l(pv); V1P = ld_frag_l(pv + 576); X4(sb, 0);  PIN(sb); SBAR(); \
      sa = MFMA32(K0N, q0[0], sa);                                              X4(sb, 4);  C2(sb, 0, pb0, 0); PIN(pb0); PIN(sb); SBAR(); \
      oa = MFMA32(V0C, as_bf8(pa0), oa);                                        X4(sb, 8);  C2(sb, 4, pb0, 2); PIN(pb0); PIN(sb); SBAR(); \
      oa = MFMA32(V1C, as_bf8(pa1), oa);                                        X4(sb, 12); C2(sb, 8, pb1, 0); PIN(pb1); PIN(sb); SBAR(); \
    } while (0)
    for (int i = 0; i < 4; i++) {
      const LAS char* pn = hb + ((c0 + i + 1) & 3) * 8 * REC;
      STEP(k0A, k1A, k0B, k1B, v0A, v1A, v0B, v1B, pk + 1 * REC, pv + vstep);
      STEP(k0B, k1B, k0A, k1A, v0B, v1B, v0A, v1A, pk + 2 * REC, pv + vstep);
      STEP(k0A, k1A, k0B, k1B, v0A, v1A, v0B, v1B, pk + 3 * REC, pv + vstep);
      STEP(k0B, k1B, k0A, k1A, v0B, v1B, v0A, v1A, pk + 4 * REC, pv + vstep);
      STEP(k0A, k1A, k0B, k1B, v0A, v1A, v0B, v1B, pk + 5 * REC, pv + vstep);
      STEP(k0B, k1B, k0A, k1A, v0B, v1B, v0A, v1A, pk + 6 * REC, pv + vstep);
      STEP(k0A, k1A, k0B, k1B, v0A, v1A, v0B, v1B, pk + 7 * REC, pv + vstep);
      if (i == 0 && attempt == 0) { wait_vm0(); wg_barrier(); }
      STEP(k0B, k1B, k0A, k1A, v0B, v1B, v0A, v1A, pn, vreal ? pn + ovn : vconst);
      pk = pn;
    }
#undef STEP
    C2(sb, 12, pb1, 2);
    ob = MFMA32(v0B, as_bf8(pb0), ob);
    ob = MFMA32(v1B, as_bf8(pb1), ob);
    const bool bad = (h == 0) && (!(oa[10] < L_LIMIT) || !(ob[10] < L_LIMIT));
    if (attempt || !__any(bad)) break;
    float ma = -3.0e38f, mb = -3.0e38f;
    const bf16x4 z1a = mk_q1(qt[0], 0.f, h), z1b = mk_q1(qt[1], 0.f, h);
    for (int T = 0; T < 32; T++) {
      const LAS char* rp = hb + T * REC;
      const bf16x8 k0 = ld_frag_l(rp + ok0); const bf16x4 k1 = mk_k1(*(const LAS unsigned*)(rp + ok1));
      f32x16 s = MFMA32K8(k1, z1a, zero); s = MFMA32(k0, q0[0], s); ma = fmaxf(ma, tile_max(s));
      s = MFMA32K8(k1, z1b, zero); s = MFMA32(k0, q0[1], s); mb = fmaxf(mb, tile_max(s));
    }
    mref[0] = bf_round(ma); mref[1] = bf_round(mb);
  }
  STAMP(8 * l + 4);
  wg_barrier();
  { const f32x16& og = kh ? oa : ob; const float mg = kh ? mref[0] : mref[1];
    LAS float* d = (LAS float*)smem + w * 13 * 64 + lane;
#pragma unroll
    for (int i = 0; i < 12; i++) d[i * 64] = og[i];
    d[12 * 64] = mg; }
  wg_barrier();
  { f32x16 o = kh ? ob : oa; const float mo = kh ? mref[1] : mref[0];
    const LAS float* s = (const LAS float*)smem + (w ^ 4) * 13 * 64 + lane;
    const float mp = s[12 * 64], mx = fmaxf(mo, mp), fo = EX(mo - mx), fp = EX(mp - mx);
#pragma unroll
    for (int i = 0; i < 12; i++) o[i] = o[i] * fo + s[i * 64] * fp;
    store_o_tile(c, OFF_OT + 2 * l * XT_BYTES, o, 64 * g + 32 * kh, 4 * m + hl);
  }
  publish(c, F_O + 2 * l);
}

template <bool LAST_OK> DI void unit_out(const Ctx& c_in, int li, bool wpre, unsigned next_w) {
  const Ctx c = fresh(c_in);
  const Params& p = c.p;
  const int lane = c.lane, w = c.w, r16 = c.r16, g4 = c.g4, g = c.g, m = c.m, ft = w & 3, th = w >> 2;
  if (!wpre) dma_wslice(c, ((li & 1) ? OFF_WOB : OFF_WOA) + m * WSL);
  const bool ex = (w < 2) || (w >= 6);
  const int te = w < 2 ? w : w - 4;
  const unsigned XO = OFF_XT + li * XT_BYTES;
  const int tt0 = 4 * g + 2 * th, cmain = 9 * m + 2 * ft + (g4 >> 1);
  const u32x2 xo0 = ld8c(c.rs, XO + ((tt0 * 72 + cmain) * 16 + r16) * 16 + (g4 & 1) * 8);
  const u32x2 xo1 = ld8c(c.rs, XO + (((tt0 + 1) * 72 + cmain) * 16 + r16) * 16 + (g4 & 1) * 8);
  u32x2 xoe = {};
  if (ex) xoe = ld8c(c.rs, XO + (((4 * g + te) * 72 + 9 * m + 8) * 16 + r16) * 16 + (g4 & 1) * 8);
  if (w == 7) poll(c, flag_off(F_O + li, g, lane & 7), lane < 8);
  if (!wpre) wait_vm0();
  wg_barrier();
  dma_tile<true>(c, OFF_OT + li * XT_BYTES + g * TILE_BYTES);
  bf16x8 W[KSTEPS], WX[KSTEPS];
  wregs_from_lds(c, W, WX, ex);
  wait_vm0();
  wg_barrier();
  if (next_w) dma_wslice(c, next_w);
  const LAS char* A = (const LAS char*)c.smem + L_A + lane * 16;
  f32x4 accx = {}, acc0 = {}, acc1 = {};
  if (ex) {
#pragma unroll
    for (int ks = 0; ks < KSTEPS; ks++) accx = MFMA16x16(WX[ks], ld_frag_l(A + (te * KSTEPS + ks) * FRAG), accx);
  }
#pragma unroll
  for (int ks = 0; ks < KSTEPS; ks++) {
    acc0 = MFMA16x16(W[ks], ld_frag_l(A + ((2 * th) * KSTEPS + ks) * FRAG), acc0);
    acc1 = MFMA16x16(W[ks], ld_frag_l(A + ((2 * th + 1) * KSTEPS + ks) * FRAG), acc1);
  }
  const unsigned a0 = cvtpk(acc0[0] + bflo(xo0[0]), acc0[1] + bfhi(xo0[0])), a1 = cvtpk(acc0[2] + bflo(xo0[1]), acc0[3] + bfhi(xo0[1]));
  const unsigned b0 = cvtpk(acc1[0] + bflo(xo1[0]), acc1[1] + bfhi(xo1[0])), b1 = cvtpk(acc1[2] + bflo(xo1[1]), acc1[3] + bfhi(xo1[1]));
  const auto x = __builtin_amdgcn_permlane16_swap(a0, b0, false, false), y = __builtin_amdgcn_permlane16_swap(a1, b1, false, false);
  const u32x4 v = {x[0], y[0], x[1], y[1]};
  const unsigned e0 = cvtpk(accx[0] + bflo(xoe[0]), accx[1] + bfhi(xoe[0])), e1 = cvtpk(accx[2] + bflo(xoe[1]), accx[3] + bfhi(xoe[1]));
  if (!LAST_OK || li < 3) {
    const unsigned XN = OFF_XT + (li + 1) * XT_BYTES;
    st16c(c.rs, XN + (((tt0 + (g4 & 1)) * 72 + cmain) * 16 + r16) * 16, v);
    if (ex && g4 < 2) st8c(c.rs, XN + (((4 * g + te) * 72 + 9 * m + 8) * 16 + r16) * 16 + g4 * 8, u32x2{e0, e1});
    publish(c, F_X + li);
  } else {
    *(u32x4*)(p.out + (size_t)((tt0 + (g4 & 1)) * 16 + r16) * DM + 72 * m + 16 * ft + 8 * (g4 >> 1)) = v;
    if (ex && g4 < 2) *(u32x2*)(p.out + (size_t)((4 * g + te) * 16 + r16) * DM + 72 * m + 64 + 4 * g4) = u32x2{e0, e1};
  }
}

DI void unit_b(const Ctx& c_in, int l) {
  const Ctx c = fresh(c_in);
  const Params& p = c.p;
  const int lane = c.lane, w = c.w, r16 = c.r16, g4 = c.g4, g = c.g, m = c.m;
  char* smem = c.smem;
  const int t0 = w < 4 ? 2 * w : (w < 6 ? 9 + 2 * (w - 4) : (w == 6 ? 8 : 13));
  const bool two = w < 6, isT = (w == 4 || w == 5 || w == 7);
  constexpr int KPRE = 5;
  bf16x8 W0[KSTEPS], W1[KSTEPS];
  const unsigned wo = OFF_WB + (m * 14 + t0) * KSTEPS * FRAG + lane * 16;
  if (w == 7) poll(c, flag_off(F_X + 2 * l, g, lane & 7), lane < 8);
#pragma unroll
  for (int ks = 0; ks < KPRE; ks++) { W0[ks] = as_bf8(ld16(c.rs, wo + ks * FRAG)); W1[ks] = two ? as_bf8(ld16(c.rs, wo + (KSTEPS + ks) * FRAG)) : bf16x8{}; }
  wg_barrier();
  dma_tile<true>(c, OFF_XT + (2 * l + 1) * XT_BYTES + g * TILE_BYTES);
#pragma unroll
  for (int ks = KPRE; ks < KSTEPS; ks++) { W0[ks] = as_bf8(ld16(c.rs, wo + ks * FRAG)); W1[ks] = two ? as_bf8(ld16(c.rs, wo + (KSTEPS + ks) * FRAG)) : bf16x8{}; }
  if (two) asm volatile("s_waitcnt vmcnt(26)" ::: "memory"); else asm volatile("s_waitcnt vmcnt(13)" ::: "memory");
  wg_barrier();
  STAMP(8 * l + 5);
  const LAS char* A = (const LAS char*)smem + L_A + lane * 16;
  f32x4 acc[2][4] = {}, cs0 = {}, cs1 = {};
  const bf16x8 one = ones8();
  float ss = 0, qq = 0;
  const int st_t = w & 3, st_p = w >> 2;
#define QB_LOOP(MM) \
  _Pragma("unroll") for (int ks = 0; ks < KSTEPS; ks++) { \
    bf16x8 x[4]; \
    _Pragma("unroll") for (int t = 0; t < 4; t++) x[t] = ld_frag_l(A + (t * KSTEPS + ks) * FRAG); \
    _Pragma("unroll") for (int t = 0; t < 4; t++) acc[0][t] = MM(W0[ks], x[t], acc[0][t]); \
    cs0 = MM(W0[ks], one, cs0); \
    if (two) { _Pragma("unroll") for (int t = 0; t < 4; t++) acc[1][t] = MM(W1[ks], x[t], acc[1][t]); cs1 = MM(W1[ks], one, cs1); } \
    if ((ks & 1) == st_p) stats_acc(ld_frag_l(A + (st_t * KSTEPS + ks) * FRAG), ss, qq); \
  }
#define MM_F(wf, xf, cc) MFMA16x16(wf, xf, cc)
#define MM_T(wf, xf, cc) MFMA16x16(xf, wf, cc)
  if (isT) { QB_LOOP(MM_T) } else { QB_LOOP(MM_F) }
#undef QB_LOOP
  ss = xrow16_sum(ss); qq = xrow16_sum(qq);
  float* st = (float*)(smem + L_STATS);
  if (g4 == 0) *(f32x2*)(st + (st_p * 64 + st_t * 16 + r16) * 2) = f32x2{ss, qq};
  wg_barrier();
  auto tok_stats = [&](int tok, float& mean, float& rstd) {
    const f32x2 a = *(const f32x2*)(st + tok * 2), b = *(const f32x2*)(st + (64 + tok) * 2);
    finish_stats(a[0] + b[0], a[1] + b[1], mean, rstd);
  };
  const unsigned KV = OFF_KVB + l * KVB_BYTES;
  LAS char* qs = (LAS char*)smem + L_QST;
  if (!isT) {
    float mean[4], rstd[4];
#pragma unroll
    for (int t = 0; t < 4; t++) tok_stats(16 * t + r16, mean[t], rstd[t]);
    if (two) {
#pragma unroll
      for (int i = 0; i < 2; i++) {
        const int tile = t0 + i; const bool isq = tile < 4; const int hl = tile & 3;
        const f32x4 cs = i ? cs1 : cs0; const float sc = isq ? QSCALE : 1.0f;
#pragma unroll
        for (int u = 0; u < 2; u++) {
          const f32x4 c0 = acc[i][2 * u], c1 = acc[i][2 * u + 1];
          const float m0 = mean[2 * u], m1 = mean[2 * u + 1], r0 = rstd[2 * u] * sc, r1 = rstd[2 * u + 1] * sc;
          const unsigned a0 = cvtpk((c0[0] - m0 * cs[0]) * r0, (c0[1] - m0 * cs[1]) * r0), a1 = cvtpk((c0[2] - m0 * cs[2]) * r0, (c0[3] - m0 * cs[3]) * r0);
          const unsigned b0 = cvtpk((c1[0] - m1 * cs[0]) * r1, (c1[1] - m1 * cs[1]) * r1), b1 = cvtpk((c1[2] - m1 * cs[2]) * r1, (c1[3] - m1 * cs[3]) * r1);
          const auto x = __builtin_amdgcn_permlane16_swap(a0, b0, false, false), y = __builtin_amdgcn_permlane16_swap(a1, b1, false, false);
          const u32x4 v = {x[0], y[0], x[1], y[1]};
          const int tl = 16 * (g4 & 1) + r16;
          if (isq) *(LAS u32x4*)(qs + (hl * 64 + 32 * u + tl) * 32 + (g4 >> 1) * 16) = v;
          else st16c(c.rs, KV + ((4 * m + hl) * 64 + 2 * g + u) * REC + ((g4 >> 1) * 32 + tl) * 16, v);
        }
      }
    } else {
      const bool isq = g4 < 2; const float sc = isq ? QSCALE : 1.0f; const int hp = 2 * (g4 & 1);
#pragma unroll
      for (int t = 0; t < 4; t++) {
        const f32x4 cc = acc[0][t]; const float mm = mean[t], rr = rstd[t] * sc;
        const unsigned a0 = cvtpk((cc[0] - mm * cs0[0]) * rr, (cc[1] - mm * cs0[1]) * rr), a1 = cvtpk((cc[2] - mm * cs0[2]) * rr, (cc[3] - mm * cs0[3]) * rr);
        if (isq) {
          *(LAS unsigned*)(qs + 8192 + (hp * 64 + 16 * t + r16) * 4) = a0;
          *(LAS unsigned*)(qs + 8192 + ((hp + 1) * 64 + 16 * t + r16) * 4) = a1;
        } else {
          const int T = 2 * g + (t >> 1), key = 16 * (t & 1) + r16;
          st4c(c.rs, KV + ((4 * m + hp) * 64 + T) * REC + REC_K1 + key * 4, a0);
          st4c(c.rs, KV + ((4 * m + hp + 1) * 64 + T) * REC + REC_K1 + key * 4, a1);
        }
      }
    }
  } else {
    const int nt = two ? 2 : 1;
#pragma unroll
    for (int i = 0; i < 2; i++) {
      if (i < nt) {
        const float cs = i ? cs1[0] : cs0[0];
#pragma unroll
        for (int u = 0; u < 2; u++) {
          unsigned pk[2][2];
#pragma unroll
          for (int tt = 0; tt < 2; tt++) {
            float v[4];
#pragma unroll
            for (int k = 0; k < 4; k++) { float mean, rstd; tok_stats(16 * (2 * u + tt) + 4 * g4 + k, mean, rstd); v[k] = (acc[i][2 * u + tt][k] - mean * cs) * rstd; }
            pk[tt][0] = cvtpk(v[0], v[1]); pk[tt][1] = cvtpk(v[2], v[3]);
          }
          const auto x = __builtin_amdgcn_permlane32_swap(pk[0][0], pk[1][0], false, false), y = __builtin_amdgcn_permlane32_swap(pk[0][1], pk[1][1], false, false);
          const u32x4 v = {x[0], y[0], x[1], y[1]};
          if (two) st16c(c.rs, KV + ((4 * m + t0 - 9 + i) * 64 + 2 * g + u) * REC + REC_V + (g4 * 18 + r16) * 16, v);
          else if (r16 < 8) st16c(c.rs, KV + ((4 * m + (r16 >> 1)) * 64 + 2 * g + u) * REC + REC_V + (g4 * 18 + 16 + (r16 & 1)) * 16, v);
        }
      }
    }
  }
  publish(c, F_KB + l);
  STAMP(8 * l + 6);
  const int r = lane & 31, h = lane >> 5, hl = w & 3, a = w >> 2;
  const int li = 2 * l + 1, head = 4 * m + hl, Tq = 2 * g + a;
  bf16x8 q0; unsigned qt;
  q_from_stage(c, hl, 32 * a + r, q0, qt);
  if (w == 7) { const int gg = g - (lane & 7); poll(c, flag_off(F_KB + l, gg < 0 ? 0 : gg, m), lane < 5 && gg >= 0); }
  wg_barrier();
  const unsigned KH = KV + head * 64 * REC;
  const unsigned ok0 = lane * 16, ok1 = REC_K1 + r * 4;
  const bool vreal = r < 18;
  const unsigned ovn = REC_V + h * 288 + r * 16, ovc = OFF_CONST + v_const_off(r, h);
  const int Tfirst = Tq - 8, T0 = Tfirst < 0 ? 0 : Tfirst;
  bf16x8 K0[9], V0[9], V1[9]; unsigned K1[9];
#pragma unroll
  for (int i = 8; i >= 0; i--) {
    const int T = Tq - 8 + i; const unsigned rp = KH + (T < 0 ? 0 : T) * REC;
    K0[i] = as_bf8(ld16c(c.rs, rp + ok0)); K1[i] = ld4c(c.rs, rp + ok1);
    const unsigned vo = vreal ? rp + ovn : ovc;
    V0[i] = as_bf8(ld16c(c.rs, vo)); V1[i] = as_bf8(ld16c(c.rs, vo + 576));
  }
  dma_wslice(c, OFF_WOB + m * WSL);
  const f32x16 zero = {};
  f32x16 mdiag, mfirst;
#pragma unroll
  for (int i = 0; i < 16; i++) { const int cc = (i & 3) + 8 * (i >> 2) + 4 * h; mdiag[i] = cc <= r ? 0.f : -1e30f; mfirst[i] = cc > r ? 0.f : -1e30f; }
  float mref;
  { f32x16 s = MFMA32K8(mk_k1(K1[8]), mk_q1(qt, 0.f, h), mdiag);
    s = MFMA32(K0[8], q0, s);
    mref = bf_round(tile_max(s)); }
  f32x16 o;
  for (int attempt = 0;; attempt++) {
    const bf16x4 q1 = mk_q1(qt, -mref, h);
    o = zero;
#pragma unroll
    for (int i = 8; i >= 0; i--) {
      if (Tq - 8 + i >= 0) {
        f32x16 s = MFMA32K8(mk_k1(K1[i]), q1, i == 8 ? mdiag : (i == 0 ? mfirst : zero));
        s = MFMA32(K0[i], q0, s);
        u32x4 p0, p1;
        X4(s, 0); X4(s, 4); X4(s, 8); X4(s, 12);
        C2(s, 0, p0, 0); C2(s, 4, p0, 2); C2(s, 8, p1, 0); C2(s, 12, p1, 2);
        o = MFMA32(V0[i], as_bf8(p0), o); o = MFMA32(V1[i], as_bf8(p1), o);
      }
    }
    const bool bad = (h == 0) && !(o[10] < L_LIMIT);
    if (attempt || !__any(bad)) break;
    float mx = -3.0e38f;
    const bf16x4 z1 = mk_q1(qt, 0.f, h);
#pragma unroll
    for (int i = 8; i >= 0; i--) {
      if (Tq - 8 + i >= 0) {
        f32x16 s = MFMA32K8(mk_k1(K1[i]), z1, i == 8 ? mdiag : (i == 0 ? mfirst : zero));
        s = MFMA32(K0[i], q0, s);
        mx = fmaxf(mx, tile_max(s));
      }
    }
    mref = bf_round(mx);
  }
  (void)T0;
  store_o_tile(c, OFF_OT + li * XT_BYTES, o, 64 * g + 32 * a, head);
  publish(c, F_O + li);
  STAMP(8 * l + 7);
}

__global__ void __launch_bounds__(512, 2) k_main(Params p, int ub, int ue) {
  extern __shared__ __attribute__((aligned(16))) char smem[];
  Ctx c;
  c.p = p; c.smem = smem; c.lds0 = (unsigned)(uintptr_t)smem;
  c.rs = __builtin_amdgcn_make_buffer_rsrc(p.ws, 0, 0x10000000, 0x00020000);
  { const int b = blockIdx.x, xcd = b & 7, idx = b >> 3; c.g = xcd * 4 + (idx >> 3); c.m = idx & 7; }
  c.lane = threadIdx.x & 63; c.w = __builtin_amdgcn_readfirstlane(threadIdx.x >> 6);
  c.r16 = c.lane & 15; c.g4 = c.lane >> 4;
  STAMP(0);
  for (int l = 0; l < 2; l++) {
    const int u = 4 * l;
    if (u >= ub && u < ue) unit_a(c, l, u == ub);
    if (u + 1 >= ub && u + 1 < ue) unit_out<false>(c, 2 * l, false, 0);
    if (u + 2 >= ub && u + 2 < ue) unit_b(c, l);
    if (u + 3 >= ub && u + 3 < ue) unit_out<true>(c, 2 * l + 1, u + 3 > ub, (l == 0 && u + 4 < ue) ? OFF_WQA + c.m * WSL : 0u);
    STAMP(8 * l + 8);
  }
}

extern "C" void kernel_launch(void* const* d_in, const int* in_sizes, int n_in, void* d_out, int out_size, void* d_ws, size_t ws_size,
                              hipStream_t stream) {
  Params p;
  p.x0 = (const unsigned short*)d_in[0]; p.wq = (const unsigned short*)d_in[1]; p.wk = (const unsigned short*)d_in[2];
  p.wv = (const unsigned short*)d_in[3]; p.woA = (const unsigned short*)d_in[4];
  p.wqkv = (const unsigned short*)d_in[6]; p.woB = (const unsigned short*)d_in[7];
  p.ws = (char*)d_ws; p.out = (unsigned short*)d_out;
  (void)hipFuncSetAttribute((const void*)k_main, hipFuncAttributeMaxDynamicSharedMemorySize, LDS_TOTAL);
  k_setup<<<256, 512, 0, stream>>>(p);
  k_main<<<256, 512, LDS_TOTAL, stream>>>(p, 0, 8);
}
```

```cpp
#include <hip/hip_runtime.h>
#include <hip/hip_bf16.h>
#include <stdint.h>

typedef short bf16x8 __attribute__((ext_vector_type(8)));
typedef short bf16x4 __attribute__((ext_vector_type(4)));
typedef __bf16 bf2_t __attribute__((ext_vector_type(2)));
typedef float f32x2 __attribute__((ext_vector_type(2)));
typedef float f32x4 __attribute__((ext_vector_type(4)));
typedef float f32x16 __attribute__((ext_vector_type(16)));
typedef unsigned u32x2 __attribute__((ext_vector_type(2)));
typedef unsigned u32x4 __attribute__((ext_vector_type(4)));
#define LAS __attribute__((address_space(3)))
#define GAS __attribute__((address_space(1)))
#define DI __device__ __forceinline__
#define MFMA16x16(a, b, c) __builtin_amdgcn_mfma_f32_16x16x32_bf16(a, b, c, 0, 0, 0)
#define MFMA32(a, b, c) __builtin_amdgcn_mfma_f32_32x32x16_bf16(a, b, c, 0, 0, 0)
#define MFMA32K8(a, b, c) __builtin_amdgcn_mfma_f32_32x32x8bf16_1k(a, b, c, 0, 0, 0)
#define SBAR() __builtin_amdgcn_sched_barrier(0)
#define PIN(x) asm volatile("" : "+v"(x))

constexpr int SEQ = 2048, DM = 576;
constexpr int KSTEPS = 18;
constexpr int FRAG = 1024;
constexpr int TILE_BYTES = 4 * KSTEPS * FRAG;
constexpr int WSL = 4 * KSTEPS * FRAG + KSTEPS * 512;
constexpr int REC = 2304;
constexpr int REC_K1 = 1024, REC_V = 1152;
constexpr float QSCALE = 0.23570226039551584f * 1.4426950408889634f;
constexpr float LN_EPS = 1e-5f;
constexpr float L_LIMIT = 1.8446744e19f;

constexpr unsigned XT_BYTES = SEQ * DM * 2;
constexpr unsigned OFF_XT = 0;
constexpr unsigned OFF_OT = OFF_XT + 4 * XT_BYTES;
constexpr unsigned KVA_BYTES = 64 * REC;
constexpr unsigned OFF_KVA = OFF_OT + 4 * XT_BYTES;
constexpr unsigned KVB_BYTES = 32 * 64 * REC;
constexpr unsigned OFF_KVB = OFF_KVA + 2 * KVA_BYTES;
constexpr unsigned OFF_WQA = OFF_KVB + 2 * KVB_BYTES;
constexpr unsigned OFF_WOA = OFF_WQA + 8 * WSL;
constexpr unsigned OFF_WOB = OFF_WOA + 8 * WSL;
constexpr unsigned OFF_WKV = OFF_WOB + 8 * WSL;
constexpr unsigned OFF_WB = OFF_WKV + 4 * KSTEPS * FRAG;
constexpr unsigned OFF_CS = OFF_WB + 8 * 14 * KSTEPS * FRAG;
constexpr int CS_KV = 8 * 5 * 16, CS_B = CS_KV + 4 * 16, CS_N = CS_B + 8 * 14 * 16;
constexpr unsigned OFF_CONST = OFF_CS + CS_N * 4;
constexpr unsigned OFF_FLAGS = OFF_CONST + 1024;
constexpr int NFL = 11, F_X = 0, F_O = 3, F_KV = 7, F_KB = 9;
constexpr unsigned FLAG_BYTES = NFL * 32 * 128;
constexpr unsigned WS_END = OFF_FLAGS + FLAG_BYTES;

constexpr int L_A = 0;
constexpr int L_W = TILE_BYTES;
constexpr int L_CONST = KVA_BYTES;
constexpr int L_QST = L_CONST + 1024;
constexpr int L_STATS = L_QST + 9216;
constexpr int L_JUNK = L_STATS + 3072;
constexpr int LDS_TOTAL = 163840;
static_assert(L_STATS >= L_W + WSL && L_STATS + 2048 + 512 <= LDS_TOTAL, "LDS map");

#define STAMP(i) do {} while (0)

struct Params {
  const unsigned short* x0;  const unsigned short* wq; const unsigned short* wk; const unsigned short* wv; const unsigned short* woA;
  const unsigned short* wqkv; const unsigned short* woB;
  char* ws; unsigned short* out;
};

typedef __amdgpu_buffer_rsrc_t rsrc_t;
DI unsigned cvtpk(float a, float b) { f32x2 v = {a, b}; bf2_t r = __builtin_convertvector(v, bf2_t); return __builtin_bit_cast(unsigned, r); }
DI float bflo(unsigned u) { return __uint_as_float(u << 16); }
DI float bfhi(unsigned u) { return __uint_as_float(u & 0xffff0000u); }
DI float dot2(unsigned a, unsigned b, float c) { return __builtin_amdgcn_fdot2_f32_bf16(__builtin_bit_cast(bf2_t, a), __builtin_bit_cast(bf2_t, b), c, false); }
DI float xrow16_sum(float x) {
  auto s = __builtin_amdgcn_permlane16_swap(__float_as_uint(x), __float_as_uint(x), false, false);
  x = __uint_as_float(s[0]) + __uint_as_float(s[1]);
  auto t = __builtin_amdgcn_permlane32_swap(__float_as_uint(x), __float_as_uint(x), false, false);
  return __uint_as_float(t[0]) + __uint_as_float(t[1]);
}
DI float xhalf_max(float x) {
  auto t = __builtin_amdgcn_permlane32_swap(__float_as_uint(x), __float_as_uint(x), false, false);
  return fmaxf(__uint_as_float(t[0]), __uint_as_float(t[1]));
}
DI float lo_half(float x) {
  auto t = __builtin_amdgcn_permlane32_swap(__float_as_uint(x), __float_as_uint(x), false, false);
  return __uint_as_float(t[0]);
}
template <bool SC1> DI void glds16(const char* base, unsigned voff, unsigned lds_base) {
  unsigned sv;
  lds_base = __builtin_amdgcn_readfirstlane(lds_base);
  if (SC1) asm volatile("s_mov_b32 %0, m0\n\ts_mov_b32 m0, %3\n\ts_nop 0\n\tglobal_load_lds_dwordx4 %1, %2 sc1\n\ts_mov_b32 m0, %0"
                        : "=&s"(sv) : "v"(voff), "s"(base), "s"(lds_base) : "memory");
  else     asm volatile("s_mov_b32 %0, m0\n\ts_mov_b32 m0, %3\n\ts_nop 0\n\tglobal_load_lds_dwordx4 %1, %2\n\ts_mov_b32 m0, %0"
                        : "=&s"(sv) : "v"(voff), "s"(base), "s"(lds_base) : "memory");
}
DI void glds4_touch(const char* base, unsigned voff, unsigned lds_base) {
  unsigned sv;
  lds_base = __builtin_amdgcn_readfirstlane(lds_base);
  asm volatile("s_mov_b32 %0, m0\n\ts_mov_b32 m0, %3\n\ts_nop 0\n\tglobal_load_lds_dword %1, %2\n\ts_mov_b32 m0, %0"
               : "=&s"(sv) : "v"(voff), "s"(base), "s"(lds_base) : "memory");
}
DI void wait_vm0() { asm volatile("s_waitcnt vmcnt(0)" ::: "memory"); }
DI void wg_barrier() { asm volatile("s_waitcnt lgkmcnt(0)\n\ts_barrier" ::: "memory"); }
DI bf16x8 ld_frag_l(const LAS char* p) { return *(const LAS bf16x8*)p; }
DI u32x4 as_u4(bf16x8 v) { return __builtin_bit_cast(u32x4, v); }
DI bf16x8 as_bf8(u32x4 v) { return __builtin_bit_cast(bf16x8, v); }
DI bf16x8 ones8() { u32x4 o = {0x3f803f80u, 0x3f803f80u, 0x3f803f80u, 0x3f803f80u}; return as_bf8(o); }
DI u32x4 ld16(rsrc_t r, unsigned off) { return __builtin_amdgcn_raw_buffer_load_b128(r, off, 0, 0); }
DI u32x4 ld16c(rsrc_t r, unsigned off) { return __builtin_amdgcn_raw_buffer_load_b128(r, off, 0, 16); }
DI u32x2 ld8c(rsrc_t r, unsigned off) { return __builtin_amdgcn_raw_buffer_load_b64(r, off, 0, 16); }
DI unsigned ld4c(rsrc_t r, unsigned off) { return __builtin_amdgcn_raw_buffer_load_b32(r, off, 0, 16); }
DI void st16c(rsrc_t r, unsigned off, u32x4 v) { __builtin_amdgcn_raw_buffer_store_b128(v, r, off, 0, 16); }
DI void st8c(rsrc_t r, unsigned off, u32x2 v) { __builtin_amdgcn_raw_buffer_store_b64(v, r, off, 0, 16); }
DI void st4c(rsrc_t r, unsigned off, unsigned v) { __builtin_amdgcn_raw_buffer_store_b32(v, r, off, 0, 16); }

constexpr int SETUP_JOBS = 256 + CS_N / 32;
DI unsigned short lds_u16(const char* smem, int byte) { return *(const LAS unsigned short*)((const LAS char*)smem + byte); }
template <class F> DI u32x4 gather8(F addr, const char* smem) {
  u32x4 v;
#pragma unroll
  for (int i = 0; i < 4; i++) v[i] = lds_u16(smem, addr(2 * i)) | ((unsigned)lds_u16(smem, addr(2 * i + 1)) << 16);
  return v;
}
DI int operm(int ks, int g4, int j) {
  const int c = 4 * ks + g4;
  return c < 64 ? 18 * (c >> 1) + 8 * (j >> 2) + 4 * (c & 1) + (j & 3) : 18 * (4 * (c - 64) + (j >> 1)) + 16 + (j & 1);
}
__global__ void __launch_bounds__(512) k_setup(Params p) {
  __shared__ __attribute__((aligned(16))) char smem[42 * 1024];
  const int job = blockIdx.x, t = threadIdx.x;

  if (job >= 256) {
    const int e = (job - 256) * 32 + (t & 31), rg = t >> 5;
    const unsigned short* src; int ld, col; bool valid = true;
    if (e < CS_KV) { const int m = e / 80, ft = (e >> 4) % 5, f = e & 15; src = p.wq; ld = DM;
      if (ft < 4) col = 18 * (4 * m + ft) + f; else { valid = f < 8; col = 18 * (4 * m + ((f & 7) >> 1)) + 16 + (f & 1); } }
    else if (e < CS_B) { const int tl = (e - CS_KV) >> 4, f = e & 15; src = (tl < 2) ? p.wk : p.wv; ld = 18;
      if (tl & 1) { valid = f < 2; col = 16 + (f & 1); } else col = f; }
    else { const int q = e - CS_B, m = q / 224, tl = (q >> 4) % 14, f = q & 15; src = p.wqkv; ld = 3 * DM;
      if (tl < 4) col = 18 * (4 * m + tl) + f;
      else if (tl < 8) col = DM + 18 * (4 * m + tl - 4) + f;
      else if (tl == 8) col = (f < 8 ? 0 : DM) + 18 * (4 * m + ((f & 7) >> 1)) + 16 + (f & 1);
      else if (tl < 13) col = 2 * DM + 18 * (4 * m + tl - 9) + f;
      else { valid = f < 8; col = 2 * DM + 18 * (4 * m + ((f & 7) >> 1)) + 16 + (f & 1); } }
    unsigned short v[36];
#pragma unroll
    for (int i = 0; i < 36; i++) v[i] = src[(size_t)(rg * 36 + i) * ld + col];
    float sum = 0.f;
#pragma unroll
    for (int i = 0; i < 36; i++) sum += __uint_as_float((unsigned)v[i] << 16);
    float* red = (float*)smem;
    red[rg * 32 + (t & 31)] = sum;
    __syncthreads();
    if (t < 32) { float a = 0.f;
#pragma unroll
      for (int i = 0; i < 16; i++) a += red[i * 32 + t];
      ((float*)(p.ws + OFF_CS))[e] = valid ? a : 0.f; }

    return;
  }
  if (job >= 128) {
    const int tt = job - 128;
    for (int c = t; c < 72 * 16; c += 512) {
      const int ch = c >> 4, r = c & 15;
      *(u32x4*)(p.ws + OFF_XT + ((size_t)tt * 72 * 16 + c) * 16) = *(const u32x4*)(p.x0 + (size_t)(tt * 16 + r) * DM + ch * 8);
    }

    return;
  }
  if (job == 127) {
    if (t < 64) { const unsigned v = (t == 11 || t == 13 || t == 47 || t == 49) ? 0x3f803f80u : 0u; *(u32x4*)(p.ws + OFF_CONST + t * 16) = u32x4{v, v, v, v}; }
    for (int i = t; i < (int)(FLAG_BYTES / 16); i += 512) *(u32x4*)(p.ws + OFF_FLAGS + i * 16) = u32x4{0, 0, 0, 0};

    return;
  }
  if (job == 126) {
    for (int c = t; c < 2 * 1296; c += 512) { const int a = c / 1296, i = c % 1296; *(LAS u32x4*)((LAS char*)smem + c * 16) = *(const u32x4*)((a ? p.wv : p.wk) + i * 8); }
    __syncthreads();
    for (int oc = t; oc < 4 * KSTEPS * 64; oc += 512) {
      const int fr = oc >> 6, lane = oc & 63, f = lane & 15, g4 = lane >> 4, tl = fr / KSTEPS, ks = fr % KSTEPS;
      const bool valid = !(tl & 1) || f < 2; const int col = (tl & 1) ? 16 + (f & 1) : f;
      u32x4 v = gather8([&](int j) { return (tl >> 1) * 20736 + ((32 * ks + 8 * g4 + j) * 18 + col) * 2; }, smem);
      if (!valid) v = u32x4{0, 0, 0, 0};
      *(u32x4*)(p.ws + OFF_WKV + (size_t)oc * 16) = v;
    }

    return;
  }
  if (job < 18 || job >= 90) {
    const bool isq = job < 18; const bool isB = job >= 108; const int ks = isq ? job : job - (isB ? 108 : 90);
    const unsigned short* src = isq ? p.wq : (isB ? p.woB : p.woA);
    for (int c = t; c < 32 * 72; c += 512) {
      const int rr = c / 72, ch = c % 72, row = isq ? 32 * ks + rr : operm(ks, rr >> 3, rr & 7);
      *(LAS u32x4*)((LAS char*)smem + c * 16) = *(const u32x4*)(src + (size_t)row * DM + ch * 8);
    }
    __syncthreads();
    char* dst = p.ws + (isq ? OFF_WQA : (isB ? OFF_WOB : OFF_WOA));
    for (int oc = t; oc < 8 * (4 * 64 + 32); oc += 512) {
      const int m = oc / 288, e = oc % 288; int f, g4, col, off;
      if (e < 256) { const int ft = e >> 6, lane = e & 63; f = lane & 15; g4 = lane >> 4; col = isq ? 18 * (4 * m + ft) + f : 72 * m + 16 * ft + f; off = (ft * KSTEPS + ks) * FRAG + lane * 16; }
      else { const int l = e - 256; f = l & 7; g4 = l >> 3; col = isq ? 18 * (4 * m + (f >> 1)) + 16 + (f & 1) : 72 * m + 64 + f; off = 4 * KSTEPS * FRAG + ks * 512 + l * 16; }
      *(u32x4*)(dst + (size_t)m * WSL + off) = gather8([&](int j) { return (8 * g4 + j) * 1152 + col * 2; }, smem);
    }

    return;
  }
  {
    const int j0 = job - 18, ks = j0 >> 2, mp = j0 & 3;
    for (int c = t; c < 32 * 54; c += 512) {
      const int rr = c / 54, e = c % 54, part = e / 18, ch = e % 18;
      *(LAS u32x4*)((LAS char*)smem + c * 16) = *(const u32x4*)(p.wqkv + (size_t)(32 * ks + rr) * (3 * DM) + part * DM + mp * 144 + ch * 8);
    }
    __syncthreads();
    for (int oc = t; oc < 2 * 14 * 64; oc += 512) {
      const int mm = oc / (14 * 64), tl = (oc >> 6) % 14, lane = oc & 63, f = lane & 15, g4 = lane >> 4;
      int part, col; bool valid = true;
      if (tl < 4) { part = 0; col = 72 * mm + 18 * tl + f; }
      else if (tl < 8) { part = 1; col = 72 * mm + 18 * (tl - 4) + f; }
      else if (tl == 8) { part = f >> 3; col = 72 * mm + 18 * ((f & 7) >> 1) + 16 + (f & 1); }
      else if (tl < 13) { part = 2; col = 72 * mm + 18 * (tl - 9) + f; }
      else { part = 2; valid = f < 8; col = 72 * mm + 18 * ((f & 7) >> 1) + 16 + (f & 1); }
      u32x4 v = gather8([&](int j) { return ((8 * g4 + j) * 54 + part * 18) * 16 + col * 2; }, smem);
      if (!valid) v = u32x4{0, 0, 0, 0};
      *(u32x4*)(p.ws + OFF_WB + ((size_t)((2 * mp + mm) * 14 + tl) * KSTEPS + ks) * FRAG + lane * 16) = v;
    }
  }

}

struct Ctx {
  Params p; rsrc_t rs; char* smem; unsigned lds0;
  int g, m, lane, w, r16, g4;
};
DI Ctx fresh(const Ctx& c0) {
  Ctx c = c0;
  asm volatile("" : "+v"(c.lane), "+s"(c.w), "+s"(c.g), "+s"(c.m), "+s"(c.lds0));
  c.r16 = c.lane & 15; c.g4 = c.lane >> 4;
  return c;
}
DI unsigned flag_off(int id, int g, int word) { return OFF_FLAGS + ((id * 32 + g) * 32 + word) * 4; }
DI void flag_set(const Ctx& c, int id, int word) { __hip_atomic_store((GAS unsigned*)(c.p.ws + flag_off(id, c.g, word)), 1u, __ATOMIC_RELAXED, __HIP_MEMORY_SCOPE_AGENT); }
DI unsigned flag_ld(const Ctx& c, unsigned off) { return __hip_atomic_load((GAS unsigned*)(c.p.ws + off), __ATOMIC_RELAXED, __HIP_MEMORY_SCOPE_AGENT); }
template <int N> DI void poll(const Ctx& c, const unsigned (&off)[N], bool need) {
  for (unsigned spins = 0; spins < (1u << 17); spins++) {
    unsigned v[N]; bool ok = true;
#pragma unroll
    for (int i = 0; i < N; i++) v[i] = need ? flag_ld(c, off[i]) : 1u;
#pragma unroll
    for (int i = 0; i < N; i++) ok &= v[i] != 0u;
    if (__all(ok)) break;
    __builtin_amdgcn_s_sleep(1);
  }
}
DI void poll1(const Ctx& c, unsigned off, bool need) { const unsigned o[1] = {off}; poll<1>(c, o, need); }
DI void publish(const Ctx& c, int id) { wait_vm0(); wg_barrier(); if (threadIdx.x == 0) flag_set(c, id, c.m); }

DI void dma_tile(const Ctx& c, unsigned src) {
#pragma unroll
  for (int i = 0; i < 9; i++) {
    const int pc = c.w + 8 * i, fr = (pc & 3) * KSTEPS + (pc >> 2);
    glds16<true>(c.p.ws, src + fr * FRAG + c.lane * 16, c.lds0 + L_A + fr * FRAG);
  }
}
DI void dma_wslice(const Ctx& c, unsigned src) {
#pragma unroll
  for (int i = 0; i < 11; i++) {
    const int fr = i < 10 ? i * 8 + c.w : 80;
    glds16<false>(c.p.ws, src + fr * FRAG + c.lane * 16, c.lds0 + L_W + fr * FRAG);
  }
}
#define WAIT_VM(n) asm volatile("s_waitcnt vmcnt(" #n ")" ::: "memory")
DI void stats_acc(bf16x8 x, float& s, float& q) {
  const u32x4 u = as_u4(x);
#pragma unroll
  for (int i = 0; i < 4; i++) { s = dot2(u[i], 0x3f803f80u, s); q = dot2(u[i], u[i], q); }
}
DI f32x2 finish_stats(float s, float q) {
  const float mean = s * (1.0f / DM);
  const float var = fmaxf(q * (1.0f / DM) - mean * mean, 0.f);
  return f32x2{mean, __builtin_amdgcn_rsqf(var + LN_EPS)};
}
constexpr int L_STAB = L_STATS + 2048;
template <int NP> DI void finalize_stats(const Ctx& c) {
  wg_barrier();
  if (c.w == 0) {
    const LAS float* st = (const LAS float*)((LAS char*)c.smem + L_STATS);
    float s = 0.f, q = 0.f;
#pragma unroll
    for (int k = 0; k < NP; k++) { const f32x2 v = *(const LAS f32x2*)(st + (k * 64 + c.lane) * 2); s += v[0]; q += v[1]; }
    *(LAS f32x2*)((LAS char*)c.smem + L_STAB + c.lane * 8) = finish_stats(s, q);
  }
  wg_barrier();
}
DI f32x2 tok_stat(const Ctx& c, int tok) { return *(const LAS f32x2*)((const LAS char*)c.smem + L_STAB + tok * 8); }
DI void wmain_from_lds(const Ctx& c, bf16x8 (&W)[KSTEPS]) {
  const LAS char* wl = (const LAS char*)c.smem + L_W + ((c.w & 3) * KSTEPS) * FRAG + c.lane * 16;
#pragma unroll
  for (int ks = 0; ks < KSTEPS; ks++) W[ks] = ld_frag_l(wl + ks * FRAG);
}
DI void wtail_from_lds(const Ctx& c, bf16x8 (&WX)[KSTEPS], int lds_off) {
  const LAS char* wl = (const LAS char*)c.smem + lds_off + (c.g4 * 8 + (c.r16 & 7)) * 16;
#pragma unroll
  for (int ks = 0; ks < KSTEPS; ks++) WX[ks] = ld_frag_l(wl + ks * 512);
}
template <bool T, int D> DI f32x4 gemm1(const LAS char* A, const bf16x8 (&W)[KSTEPS]) {
  f32x4 acc = {}; bf16x8 x[KSTEPS];
#pragma unroll
  for (int ks = 0; ks < D; ks++) x[ks] = ld_frag_l(A + ks * FRAG);
  SBAR();
#pragma unroll
  for (int ks = 0; ks < KSTEPS; ks++) {
    if (ks + D < KSTEPS) x[ks + D] = ld_frag_l(A + (ks + D) * FRAG);
    acc = T ? MFMA16x16(x[ks], W[ks], acc) : MFMA16x16(W[ks], x[ks], acc);
    SBAR();
  }
  return acc;
}
template <int EX, bool CHUNK, int NY, int NM, class Mid> DI void gemm2(const LAS char* A0, const bf16x8 (&W)[KSTEPS], const bf16x8 (&WX)[KSTEPS], f32x4& acc0, f32x4& acc1, f32x4& accx, Mid mid) {
  constexpr int D = 3;
  bf16x8 x0[KSTEPS], x1[KSTEPS];
  const LAS char* A1 = A0 + KSTEPS * FRAG;
  if (CHUNK) { asm volatile("s_waitcnt vmcnt(%0)" :: "n"(6 + NY) : "memory"); wg_barrier(); }
#pragma unroll
  for (int ks = 0; ks < D; ks++) { x0[ks] = ld_frag_l(A0 + ks * FRAG); x1[ks] = ld_frag_l(A1 + ks * FRAG); }
  SBAR();
#pragma unroll
  for (int ks = 0; ks < KSTEPS; ks++) {
    if (CHUNK && ks + D == 6) { asm volatile("s_waitcnt vmcnt(%0)" :: "n"(3 + NY) : "memory"); wg_barrier(); }
    if (CHUNK && ks + D == 12) { asm volatile("s_waitcnt vmcnt(%0)" :: "n"(NY + (NM < 6 ? NM : 6)) : "memory"); wg_barrier(); }
    if (ks >= 3 && ks - 3 < NM) mid(ks - 3);
    if (ks + D < KSTEPS) { x0[ks + D] = ld_frag_l(A0 + (ks + D) * FRAG); x1[ks + D] = ld_frag_l(A1 + (ks + D) * FRAG); }
    acc0 = MFMA16x16(W[ks], x0[ks], acc0);
    acc1 = MFMA16x16(W[ks], x1[ks], acc1);
    if (EX == 1) accx = MFMA16x16(WX[ks], x0[ks], accx);
    if (EX == 2) accx = MFMA16x16(WX[ks], x1[ks], accx);
    SBAR();
  }
}

DI float tile_max(const f32x16& s) {
  float a = fmaxf(fmaxf(s[0], s[1]), s[2]), b = fmaxf(fmaxf(s[3], s[4]), s[5]);
#pragma unroll
  for (int i = 6; i < 16; i += 2) { a = fmaxf(a, s[i]); b = fmaxf(b, s[i + 1]); }
  return xhalf_max(fmaxf(a, b));
}
DI bf16x4 mk_q1(unsigned tail, float negm, int h) {
  const unsigned mb = cvtpk(negm, 0.f);
  u32x2 t = {h ? 0u : tail, h ? 0u : mb};
  return __builtin_bit_cast(bf16x4, t);
}
DI bf16x4 mk_k1(unsigned tail) { u32x2 t = {tail, 0x00003f80u}; return __builtin_bit_cast(bf16x4, t); }
DI float bf_round(float x) { return bflo(cvtpk(x, 0.f)); }
DI int v_const_off(int r, int h) { return r == 18 ? (11 + 2 * h) * 16 : ((r >= 20 && r < 28) ? (12 + 2 * h) * 16 : (10 + 2 * h) * 16); }
#define EX(x) __builtin_amdgcn_exp2f(x)
#define X4(S, o) do { S[o] = EX(S[o]); S[o + 1] = EX(S[o + 1]); S[o + 2] = EX(S[o + 2]); S[o + 3] = EX(S[o + 3]); } while (0)
#define C2(S, o, P, pi) do { P[pi] = cvtpk(S[o], S[o + 1]); P[pi + 1] = cvtpk(S[o + 2], S[o + 3]); } while (0)

DI void q_from_stage(const Ctx& c, int hl, int tok, bf16x8& q0, unsigned& qt) {
  const LAS char* qs = (const LAS char*)c.smem + L_QST;
  q0 = ld_frag_l(qs + (hl * 64 + tok) * 32 + (c.lane >> 5) * 16);
  qt = *(const LAS unsigned*)(qs + 8192 + (hl * 64 + tok) * 4);
}
DI void combine_store(const Ctx& c, const f32x16& oa, const f32x16& ob, float ma, float mb, unsigned OT) {
  const int lane = c.lane, w = c.w, kh = w >> 2, r = lane & 31, h = lane >> 5, head = 4 * c.m + (w & 3);
  wg_barrier();
  { const f32x16& og = kh ? oa : ob;
    LAS float* d = (LAS float*)c.smem + w * 13 * 64 + lane;
#pragma unroll
    for (int i = 0; i < 12; i++) d[i * 64] = og[i];
    d[12 * 64] = kh ? ma : mb; }
  wg_barrier();
  f32x16 o = kh ? ob : oa; const float mo = kh ? mb : ma;
  const LAS float* s = (const LAS float*)c.smem + (w ^ 4) * 13 * 64 + lane;
  const float mp = s[12 * 64], mx = fmaxf(mo, mp), fo = EX(mo - mx), fp = EX(mp - mx);
#pragma unroll
  for (int i = 0; i < 12; i++) o[i] = o[i] * fo + s[i * 64] * fp;
  const float inv = __builtin_amdgcn_rcpf(lo_half(o[10]));
  const int tok = 64 * c.g + 32 * kh + r, tt = tok >> 4, rr = tok & 15;
  const u32x4 v = {cvtpk(o[0] * inv, o[1] * inv), cvtpk(o[2] * inv, o[3] * inv), cvtpk(o[4] * inv, o[5] * inv), cvtpk(o[6] * inv, o[7] * inv)};
  st16c(c.rs, OT + ((tt * 72 + 2 * head + h) * 16 + rr) * 16, v);
  if (h == 0) st4c(c.rs, OT + ((tt * 72 + 64 + c.m) * 16 + rr) * 16 + (head & 3) * 4, cvtpk(o[8] * inv, o[9] * inv));
}

DI void unit_a(const Ctx& c_in, int l, bool first, bf16x8 (&WOUT)[KSTEPS]) {
  const Ctx c = fresh(c_in);
  const Params& p = c.p;
  const int lane = c.lane, w = c.w, r16 = c.r16, g4 = c.g4, g = c.g, m = c.m, ft = w & 3, th = w >> 2;
  char* smem = c.smem;
  if (first) dma_wslice(c, OFF_WQA + m * WSL);
  const int tkv = m >> 1;
  int exk = 0, te = 0;
  if (w == 0 || w == 1) { exk = 1; te = w; } else if (w == 6 || w == 7) { exk = 1; te = w - 4; }
  else if (th == (tkv >> 1)) { exk = (w & 1) ? 3 : 2; te = tkv; }
  const bool exT = exk >= 2 && (m & 1);
  const int kvt = (m & 1) * 2 + (exk - 2);
  if (l > 0 && w == 7) poll1(c, flag_off(F_X + 2 * l - 1, g, lane & 7), lane < 8);
  bf16x8 W[KSTEPS], WX[KSTEPS];
  if (exk >= 2) {
#pragma unroll
    for (int ks = 0; ks < KSTEPS; ks++) WX[ks] = as_bf8(ld16(c.rs, OFF_WKV + (kvt * KSTEPS + ks) * FRAG + lane * 16));
  }
  const float* CS = (const float*)(p.ws + OFF_CS);
  const f32x4 cs = *(const f32x4*)(CS + (m * 5 + ft) * 16 + 4 * g4);
  f32x4 csx = {};
  if (exk == 1) csx = *(const f32x4*)(CS + (m * 5 + 4) * 16 + 4 * g4);
  else if (exk >= 2) { if (exT) { const float v = CS[CS_KV + kvt * 16 + r16]; csx = f32x4{v, v, v, v}; } else csx = *(const f32x4*)(CS + CS_KV + kvt * 16 + 4 * g4); }
  if (first) wait_vm0();
  wg_barrier();
  STAMP(30 * l + 1);
  dma_tile(c, OFF_XT + 2 * l * XT_BYTES + g * TILE_BYTES);
  wmain_from_lds(c, W);
  if (exk == 1) wtail_from_lds(c, WX, L_W + 4 * KSTEPS * FRAG);
  wait_vm0();
  wg_barrier();
  STAMP(30 * l + 2);
  const LAS char* A = (const LAS char*)smem + L_A + lane * 16;
  { bf16x8 xs[10];
#pragma unroll
    for (int i = 0; i < 5; i++) { const int ks = (i < 4 || ft < 2) ? 4 * i + ft : ft;
      xs[2 * i] = ld_frag_l(A + ((2 * th) * KSTEPS + ks) * FRAG); xs[2 * i + 1] = ld_frag_l(A + ((2 * th + 1) * KSTEPS + ks) * FRAG); }
    float s0 = 0, q0 = 0, s1 = 0, q1 = 0;
#pragma unroll
    for (int i = 0; i < 4; i++) { stats_acc(xs[2 * i], s0, q0); stats_acc(xs[2 * i + 1], s1, q1); }
    if (ft < 2) { stats_acc(xs[8], s0, q0); stats_acc(xs[9], s1, q1); }
    s0 = xrow16_sum(s0); q0 = xrow16_sum(q0); s1 = xrow16_sum(s1); q1 = xrow16_sum(q1);
    LAS float* st = (LAS float*)((LAS char*)smem + L_STATS);
    if (g4 == 0) {
      *(LAS f32x2*)(st + (ft * 64 + (2 * th) * 16 + r16) * 2) = f32x2{s0, q0};
      *(LAS f32x2*)(st + (ft * 64 + (2 * th + 1) * 16 + r16) * 2) = f32x2{s1, q1};
    } }
  f32x4 accx = {};
  if (exk) { if (exT) accx = gemm1<true, 6>(A + te * KSTEPS * FRAG, WX); else accx = gemm1<false, 6>(A + te * KSTEPS * FRAG, WX); }
  finalize_stats<4>(c);
  STAMP(30 * l + 3);
  LAS char* qs = (LAS char*)smem + L_QST;
  unsigned qtail0 = 0, qtail1 = 0;
  if (exk) {
    const unsigned rec = OFF_KVA + l * KVA_BYTES + ((4 * g + te) >> 1) * REC; const int sh = te & 1;
    if (!exT) {
      const f32x2 s = tok_stat(c, 16 * te + r16); const float mean = s[0], rstd = exk == 1 ? s[1] * QSCALE : s[1];
      const unsigned a0 = cvtpk((accx[0] - mean * csx[0]) * rstd, (accx[1] - mean * csx[1]) * rstd), a1 = cvtpk((accx[2] - mean * csx[2]) * rstd, (accx[3] - mean * csx[3]) * rstd);
      if (exk == 1) { qtail0 = a0; qtail1 = a1; }
      else if (exk == 2) st8c(c.rs, rec + ((g4 >> 1) * 32 + 16 * sh + r16) * 16 + (g4 & 1) * 8, u32x2{a0, a1});
      else if (g4 == 0) st4c(c.rs, rec + REC_K1 + (16 * sh + r16) * 4, a0);
    } else {
      float v[4];
#pragma unroll
      for (int i = 0; i < 4; i++) { const f32x2 s = tok_stat(c, 16 * te + 4 * g4 + i); v[i] = (accx[i] - s[0] * csx[i]) * s[1]; }
      const int d = (exk == 2) ? r16 : 16 + r16;
      if (exk == 2 || r16 < 2) st8c(c.rs, rec + REC_V + ((sh * 2 + (g4 & 1)) * 18 + d) * 16 + (g4 >> 1) * 8, u32x2{cvtpk(v[0], v[1]), cvtpk(v[2], v[3])});
    }
    if (exk >= 2) { wait_vm0(); if (lane == 0) flag_set(c, F_KV + l, 2 * m + (exk - 2)); }
  }
  STAMP(30 * l + 4);
  { f32x4 acc0 = {}, acc1 = {}, dummy = {};
    gemm2<0, false, 0, 0>(A + (2 * th) * KSTEPS * FRAG, W, W, acc0, acc1, dummy, [](int) {});
    const f32x2 s0 = tok_stat(c, (2 * th) * 16 + r16), s1 = tok_stat(c, (2 * th + 1) * 16 + r16);
    const float m0 = s0[0], r0 = s0[1] * QSCALE, m1 = s1[0], r1 = s1[1] * QSCALE;
    const unsigned a0 = cvtpk((acc0[0] - m0 * cs[0]) * r0, (acc0[1] - m0 * cs[1]) * r0), a1 = cvtpk((acc0[2] - m0 * cs[2]) * r0, (acc0[3] - m0 * cs[3]) * r0);
    const unsigned b0 = cvtpk((acc1[0] - m1 * cs[0]) * r1, (acc1[1] - m1 * cs[1]) * r1), b1 = cvtpk((acc1[2] - m1 * cs[2]) * r1, (acc1[3] - m1 * cs[3]) * r1);
    const auto x = __builtin_amdgcn_permlane16_swap(a0, b0, false, false), y = __builtin_amdgcn_permlane16_swap(a1, b1, false, false);
    *(LAS u32x4*)(qs + (ft * 64 + 16 * (2 * th + (g4 & 1)) + r16) * 32 + (g4 >> 1) * 16) = u32x4{x[0], y[0], x[1], y[1]};
    if (exk == 1 && g4 < 2) {
      *(LAS unsigned*)(qs + 8192 + ((2 * g4) * 64 + 16 * te + r16) * 4) = qtail0;
      *(LAS unsigned*)(qs + 8192 + ((2 * g4 + 1) * 64 + 16 * te + r16) * 4) = qtail1;
    } }
  if (w == 5) { const unsigned v = (lane == 11 || lane == 13 || lane == 47 || lane == 49) ? 0x3f803f80u : 0u; *(LAS u32x4*)((LAS char*)smem + L_CONST + lane * 16) = u32x4{v, v, v, v}; }
  STAMP(30 * l + 5);
  const int r = lane & 31, h = lane >> 5, hl = w & 3, kh = w >> 2;
  const int c0 = (blockIdx.x & 3);
  const unsigned KV = OFF_KVA + l * KVA_BYTES;
  if (w == 0) {
    const unsigned o[2] = {flag_off(F_KV + l, 4 * c0 + g4, r16), flag_off(F_KV + l, 16 + 4 * c0 + g4, r16)};
    poll<2>(c, o, true);
  }
  wg_barrier();
  STAMP(30 * l + 6);
  auto dma_piece = [&](int j, int i) {
    int pc = w + 8 * i; if (pc >= 36) pc -= 4;
    const unsigned o = (pc >= 18 ? 32 * REC : 0) + ((c0 + j) & 3) * 8 * REC + (pc >= 18 ? pc - 18 : pc) * 1024;
    glds16<true>(p.ws, KV + o + lane * 16, c.lds0 + o);
  };
#pragma unroll
  for (int i = 0; i < 5; i++) dma_piece(0, i);
  bf16x8 q0[2]; unsigned qt[2];
#pragma unroll
  for (int a = 0; a < 2; a++) q_from_stage(c, hl, 32 * a + r, q0[a], qt[a]);
  if (w == 0) {
    unsigned o[6];
#pragma unroll
    for (int i = 0; i < 3; i++) { o[2 * i] = flag_off(F_KV + l, 4 * ((c0 + 1 + i) & 3) + g4, r16); o[2 * i + 1] = flag_off(F_KV + l, 16 + 4 * ((c0 + 1 + i) & 3) + g4, r16); }
    poll<6>(c, o, true);
  }
  wait_vm0();
  wg_barrier();
  STAMP(30 * l + 7);
#pragma unroll
  for (int i = 0; i < 5; i++) dma_piece(1, i);
  const unsigned wo = OFF_WOA + m * WSL;
  auto ldw = [&](int ks) { WOUT[ks] = as_bf8(ld16(c.rs, wo + (ft * KSTEPS + ks) * FRAG + lane * 16)); };
  auto dma_tail = [&](int i) { const int pc = i ? 8 : w; glds16<false>(p.ws, wo + 4 * KSTEPS * FRAG + pc * 1024 + lane * 16, c.lds0 + L_QST + pc * 1024); };
  const int slot32 = (blockIdx.x >> 3) * 8 + w;
  auto warm = [&](unsigned base, unsigned bytes) {
    const unsigned o = (slot32 * 64 + lane) * 128;
    glds4_touch(p.ws, base + (o < bytes ? o : 0), c.lds0 + L_JUNK);
  };
  STAMP(30 * l + 8);
  const f32x16 zero = {};
  const LAS char* l0 = (const LAS char*)smem;
  const LAS char* hb = l0 + kh * 32 * REC;
  const int ok0 = lane * 16, ok1 = REC_K1 + r * 4;
  const bool vreal = r < 18;
  const int ovn = REC_V + h * 288 + r * 16;
  const LAS char* vconst = l0 + L_CONST + v_const_off(r, h);
  const int vstep = vreal ? REC : 0;
  float mref[2];
#pragma unroll
  for (int a = 0; a < 2; a++) {
    const LAS char* rp = hb + c0 * 8 * REC;
    f32x16 s = MFMA32K8(mk_k1(*(const LAS unsigned*)(rp + ok1)), mk_q1(qt[a], 0.f, h), zero);
    s = MFMA32(ld_frag_l(rp + ok0), q0[a], s);
    mref[a] = bf_round(tile_max(s));
  }
  f32x16 oa, ob;
#define STEP(K0C, K1C, K0N, K1N, V0C, V1C, V0P, V1P, PN, PVN, HOOK) do { \
      SBAR(); \
      C2(sb, 12, pb1, 2); PIN(pb1); SBAR(); sb = MFMA32K8(K1C, q1b, zero);      X4(sa, 0);  PIN(sa); SBAR(); \
      sb = MFMA32(K0C, q0[1], sb);  K0N = ld_frag_l((PN) + ok0); K1N = mk_k1(*(const LAS unsigned*)((PN) + ok1)); X4(sa, 4);  C2(sa, 0, pa0, 0); PIN(pa0); PIN(sa); SBAR(); \
      ob = MFMA32(V0P, as_bf8(pb0), ob);                                        X4(sa, 8);  C2(sa, 4, pa0, 2); PIN(pa0); PIN(sa); SBAR(); \
      ob = MFMA32(V1P, as_bf8(pb1), ob);  HOOK;                                 X4(sa, 12); C2(sa, 8, pa1, 0); PIN(pa1); PIN(sa); SBAR(); \
      C2(sa, 12, pa1, 2); PIN(pa1); SBAR(); sa = MFMA32K8(K1N, q1a, zero);  pv = (PVN); V0P = ld_frag_l(pv); V1P = ld_frag_l(pv + 576); X4(sb, 0);  PIN(sb); SBAR(); \
      sa = MFMA32(K0N, q0[0], sa);                                              X4(sb, 4);  C2(sb, 0, pb0, 0); PIN(pb0); PIN(sb); SBAR(); \
      oa = MFMA32(V0C, as_bf8(pa0), oa);                                        X4(sb, 8);  C2(sb, 4, pb0, 2); PIN(pb0); PIN(sb); SBAR(); \
      oa = MFMA32(V1C, as_bf8(pa1), oa);                                        X4(sb, 12); C2(sb, 8, pb1, 0); PIN(pb1); PIN(sb); SBAR(); \
    } while (0)
#define CHUNK(J, H0, H1, H2, H3, H4, H5, H6, MID, H7) do { \
      const LAS char* pn = hb + ((c0 + (J) + 1) & 3) * 8 * REC;     \
      STEP(k0A, k1A, k0B, k1B, v0A, v1A, v0B, v1B, pk + 1 * REC, pv + vstep, H0); \
      STEP(k0B, k1B, k0A, k1A, v0B, v1B, v0A, v1A, pk + 2 * REC, pv + vstep, H1); \
      STEP(k0A, k1A, k0B, k1B, v0A, v1A, v0B, v1B, pk + 3 * REC, pv + vstep, H2); \
      STEP(k0B, k1B, k0A, k1A, v0B, v1B, v0A, v1A, pk + 4 * REC, pv + vstep, H3); \
      STEP(k0A, k1A, k0B, k1B, v0A, v1A, v0B, v1B, pk + 5 * REC, pv + vstep, H4); \
      STEP(k0B, k1B, k0A, k1A, v0B, v1B, v0A, v1A, pk + 6 * REC, pv + vstep, H5); \
      STEP(k0A, k1A, k0B, k1B, v0A, v1A, v0B, v1B, pk + 7 * REC, pv + vstep, H6); \
      MID; \
      STEP(k0B, k1B, k0A, k1A, v0B, v1B, v0A, v1A, pn, vreal ? pn + ovn : vconst, H7); \
      pk = pn; \
    } while (0)
#define PASS_BEGIN() \
    const bf16x4 q1a = mk_q1(qt[0], -mref[0], h), q1b = mk_q1(qt[1], -mref[1], h); \
    const LAS char* pk = hb + c0 * 8 * REC; \
    const LAS char* pv = vreal ? pk + ovn : vconst; \
    oa = zero; ob = zero; \
    bf16x8 k0A = ld_frag_l(pk + ok0); bf16x4 k1A = mk_k1(*(const LAS unsigned*)(pk + ok1)); \
    bf16x8 k0B, v0A = ld_frag_l(pv), v1A = ld_frag_l(pv + 576), v0B = {}, v1B = {}; bf16x4 k1B; \
    f32x16 sa = MFMA32K8(k1A, q1a, zero); sa = MFMA32(k0A, q0[0], sa); \
    f32x16 sb = {}; \
    u32x4 pa0 = {}, pa1 = {}, pb0 = {}, pb1 = {};
#define PASS_END() \
    C2(sb, 12, pb1, 2);     \
    ob = MFMA32(v0B, as_bf8(pb0), ob); \
    ob = MFMA32(v1B, as_bf8(pb1), ob);
#define NOH (void)0
  {
    PASS_BEGIN()
    CHUNK(0, dma_piece(2, 0), dma_piece(2, 1), dma_piece(2, 2), dma_piece(2, 3), dma_piece(2, 4), dma_tail(0), dma_tail(1), WAIT_VM(7); wg_barrier(), warm(OFF_WB, 8 * 14 * KSTEPS * FRAG));
    CHUNK(1, dma_piece(3, 0), dma_piece(3, 1), dma_piece(3, 2), dma_piece(3, 3), dma_piece(3, 4), ldw(0), ldw(1), WAIT_VM(7); wg_barrier(), warm(OFF_WOB, 8 * WSL));
    CHUNK(2, ldw(2), ldw(3), ldw(4), ldw(5), ldw(6), ldw(7), ldw(8), WAIT_VM(7); wg_barrier(), ldw(9));
    CHUNK(3, ldw(10), ldw(11), ldw(12), ldw(13), ldw(14), ldw(15), ldw(16), NOH, ldw(17));
    PASS_END()
  }
  if (__any((h == 0) && (!(oa[10] < L_LIMIT) || !(ob[10] < L_LIMIT)))) {
    float ma = -3.0e38f, mb = -3.0e38f;
    const bf16x4 z1a = mk_q1(qt[0], 0.f, h), z1b = mk_q1(qt[1], 0.f, h);
    for (int T = 0; T < 32; T++) {
      const LAS char* rp = hb + T * REC;
      const bf16x8 k0 = ld_frag_l(rp + ok0); const bf16x4 k1 = mk_k1(*(const LAS unsigned*)(rp + ok1));
      f32x16 s = MFMA32K8(k1, z1a, zero); s = MFMA32(k0, q0[0], s); ma = fmaxf(ma, tile_max(s));
      s = MFMA32K8(k1, z1b, zero); s = MFMA32(k0, q0[1], s); mb = fmaxf(mb, tile_max(s));
    }
    mref[0] = bf_round(ma); mref[1] = bf_round(mb);
    PASS_BEGIN()
    for (int i = 0; i < 4; i++) CHUNK(i, NOH, NOH, NOH, NOH, NOH, NOH, NOH, NOH, NOH);
    PASS_END()
  }
#undef STEP
#undef CHUNK
#undef PASS_BEGIN
#undef PASS_END
  STAMP(30 * l + 9);
  combine_store(c, oa, ob, mref[0], mref[1], OFF_OT + 2 * l * XT_BYTES);
  STAMP(30 * l + 11);
  publish(c, F_O + 2 * l);
  STAMP(30 * l + 12);
}

template <int WSRC, bool LAST_OK, bool NEXTW> DI void unit_out(const Ctx& c_in, int li, unsigned next_w, bf16x8 (&W)[KSTEPS]) {
  const Ctx c = fresh(c_in);
  const Params& p = c.p;
  const int lane = c.lane, w = c.w, r16 = c.r16, g4 = c.g4, g = c.g, m = c.m, ft = w & 3, th = w >> 2;
  if (WSRC == 0) dma_wslice(c, ((li & 1) ? OFF_WOB : OFF_WOA) + m * WSL);
  const bool ex = (w < 2) || (w >= 6);
  const int te = w < 2 ? w : w - 4;
  const unsigned XO = OFF_XT + li * XT_BYTES;
  const int tt0 = 4 * g + 2 * th, cmain = 9 * m + 2 * ft + (g4 >> 1);
  if (w == 7) poll1(c, flag_off(F_O + li, g, lane & 7), lane < 8);
  u32x2 xo0 = ld8c(c.rs, XO + ((tt0 * 72 + cmain) * 16 + r16) * 16 + (g4 & 1) * 8);
  u32x2 xo1 = ld8c(c.rs, XO + (((tt0 + 1) * 72 + cmain) * 16 + r16) * 16 + (g4 & 1) * 8);
  u32x2 xoe = ld8c(c.rs, XO + (((4 * g + (ex ? te : 2 * th)) * 72 + 9 * m + 8) * 16 + r16) * 16 + (g4 & 1) * 8);
  if (WSRC == 0) wait_vm0();
  wg_barrier();
  STAMP(30 * (li >> 1) + 13 * ((li & 1) ? 2 : 1) + 0);
  PIN(xo0); PIN(xo1); PIN(xoe);
  if (WSRC == 2) {
#pragma unroll
    for (int ks = 0; ks < KSTEPS; ks++) PIN(W[ks]);
  }
  dma_tile(c, OFF_OT + li * XT_BYTES + g * TILE_BYTES);
  bf16x8 WX[KSTEPS];
  if (WSRC != 2) wmain_from_lds(c, W);
  if (ex) wtail_from_lds(c, WX, WSRC == 2 ? L_QST : L_W + 4 * KSTEPS * FRAG);
  const LAS char* A = (const LAS char*)c.smem + L_A + (2 * th) * KSTEPS * FRAG + lane * 16;
  f32x4 accx = {}, acc0 = {}, acc1 = {};
  constexpr int NM = NEXTW ? 11 : 0;
  auto mid = [&](int i) { const int fr = i < 10 ? i * 8 + w : 80;
    glds16<false>(p.ws, next_w + fr * FRAG + lane * 16, c.lds0 + L_W + fr * FRAG); };
  if (!ex) gemm2<0, true, 0, NM>(A, W, WX, acc0, acc1, accx, mid);
  else if (te & 1) gemm2<2, true, 0, NM>(A, W, WX, acc0, acc1, accx, mid);
  else gemm2<1, true, 0, NM>(A, W, WX, acc0, acc1, accx, mid);
  STAMP(30 * (li >> 1) + 13 * ((li & 1) ? 2 : 1) + 2);
  const unsigned a0 = cvtpk(acc0[0] + bflo(xo0[0]), acc0[1] + bfhi(xo0[0])), a1 = cvtpk(acc0[2] + bflo(xo0[1]), acc0[3] + bfhi(xo0[1]));
  const unsigned b0 = cvtpk(acc1[0] + bflo(xo1[0]), acc1[1] + bfhi(xo1[0])), b1 = cvtpk(acc1[2] + bflo(xo1[1]), acc1[3] + bfhi(xo1[1]));
  const auto x = __builtin_amdgcn_permlane16_swap(a0, b0, false, false), y = __builtin_amdgcn_permlane16_swap(a1, b1, false, false);
  const u32x4 v = {x[0], y[0], x[1], y[1]};
  const unsigned e0 = cvtpk(accx[0] + bflo(xoe[0]), accx[1] + bfhi(xoe[0])), e1 = cvtpk(accx[2] + bflo(xoe[1]), accx[3] + bfhi(xoe[1]));
  if (!LAST_OK || li < 3) {
    const unsigned XN = OFF_XT + (li + 1) * XT_BYTES;
    st16c(c.rs, XN + (((tt0 + (g4 & 1)) * 72 + cmain) * 16 + r16) * 16, v);
    if (ex && g4 < 2) st8c(c.rs, XN + (((4 * g + te) * 72 + 9 * m + 8) * 16 + r16) * 16 + g4 * 8, u32x2{e0, e1});
    publish(c, F_X + li);
    STAMP(30 * (li >> 1) + 13 * ((li & 1) ? 2 : 1) + 3);
  } else {
    *(u32x4*)(p.out + (size_t)((tt0 + (g4 & 1)) * 16 + r16) * DM + 72 * m + 16 * ft + 8 * (g4 >> 1)) = v;
    if (ex && g4 < 2) *(u32x2*)(p.out + (size_t)((4 * g + te) * 16 + r16) * DM + 72 * m + 64 + 4 * g4) = u32x2{e0, e1};
  }
}

template <bool T, bool TWO> DI void qb_loop(const Ctx& c, int sb, unsigned xsrc, unsigned wo, bf16x8 (&W0)[KSTEPS], bf16x8 (&W1)[KSTEPS], f32x4 (&acc)[2][4],
                                            int st_t, int st_p, float& ss, float& qq) {
  const Params& p = c.p;
  constexpr int NW = TWO ? 2 : 1;
  auto xpiece = [&](int i) { const int pc = c.w + 8 * i, fr = (pc & 3) * KSTEPS + (pc >> 2);
    glds16<true>(p.ws, xsrc + fr * FRAG + c.lane * 16, c.lds0 + L_A + fr * FRAG); };
  auto wload = [&](int ks) { W0[ks] = as_bf8(ld16(c.rs, wo + ks * FRAG)); if (TWO) W1[ks] = as_bf8(ld16(c.rs, wo + (KSTEPS + ks) * FRAG)); };
  const LAS char* A = (const LAS char*)c.smem + L_A + c.lane * 16;
  const LAS char* As = A + (st_t * KSTEPS + st_p) * FRAG;
  bf16x8 x[KSTEPS][4], xs;
  xpiece(0); xpiece(1); xpiece(2); wload(2); wload(3);
  asm volatile("s_waitcnt vmcnt(%0)" :: "n"(2 * NW) : "memory"); wg_barrier();
  STAMP(sb + 0);
#pragma unroll
  for (int t = 0; t < 4; t++) x[0][t] = ld_frag_l(A + (t * KSTEPS + 0) * FRAG);
#pragma unroll
  for (int t = 0; t < 4; t++) x[1][t] = ld_frag_l(A + (t * KSTEPS + 1) * FRAG);
  SBAR();
#pragma unroll
  for (int ks = 0; ks < KSTEPS; ks++) {
    if (ks == 4) { asm volatile("s_waitcnt vmcnt(%0)" :: "n"(2 * NW + 1) : "memory"); wg_barrier(); STAMP(sb + 1); }
    if (ks == 10) { asm volatile("s_waitcnt vmcnt(%0)" :: "n"(5 * NW) : "memory"); wg_barrier(); STAMP(sb + 2); }
    if (ks < 6) xpiece(3 + ks);
    if (ks + 4 < KSTEPS) wload(ks + 4);
    if (!(ks & 1)) xs = ld_frag_l(As + ks * FRAG);
    if (ks + 2 < KSTEPS) {
#pragma unroll
      for (int t = 0; t < 4; t++) x[ks + 2][t] = ld_frag_l(A + (t * KSTEPS + ks + 2) * FRAG);
    }
#pragma unroll
    for (int t = 0; t < 4; t++) acc[0][t] = T ? MFMA16x16(x[ks][t], W0[ks], acc[0][t]) : MFMA16x16(W0[ks], x[ks][t], acc[0][t]);
    if (TWO) {
#pragma unroll
      for (int t = 0; t < 4; t++) acc[1][t] = T ? MFMA16x16(x[ks][t], W1[ks], acc[1][t]) : MFMA16x16(W1[ks], x[ks][t], acc[1][t]);
    }
    if (ks & 1) stats_acc(xs, ss, qq);
    SBAR();
  }
}
DI void unit_b(const Ctx& c_in, int l) {
  const Ctx c = fresh(c_in);
  const Params& p = c.p;
  const int lane = c.lane, w = c.w, r16 = c.r16, g4 = c.g4, g = c.g, m = c.m;
  char* smem = c.smem;
  const int t0 = w < 4 ? 2 * w : (w < 6 ? 9 + 2 * (w - 4) : (w == 6 ? 8 : 13));
  const bool two = w < 6, isT = (w == 4 || w == 5 || w == 7);
  bf16x8 W0[KSTEPS], W1[KSTEPS];
  const unsigned wo = OFF_WB + (m * 14 + t0) * KSTEPS * FRAG + lane * 16;
  if (w == 7) poll1(c, flag_off(F_X + 2 * l, g, lane & 7), lane < 8);
#pragma unroll
  for (int ks = 0; ks < 2; ks++) { W0[ks] = as_bf8(ld16(c.rs, wo + ks * FRAG)); if (two) W1[ks] = as_bf8(ld16(c.rs, wo + (KSTEPS + ks) * FRAG)); }
  const float* CS = (const float*)(p.ws + OFF_CS) + CS_B + (m * 14 + t0) * 16;
  f32x4 cs0, cs1;
  if (isT) { const float a = CS[r16], b = CS[16 + r16]; cs0 = f32x4{a, a, a, a}; cs1 = f32x4{b, b, b, b}; }
  else { cs0 = *(const f32x4*)(CS + 4 * g4); cs1 = *(const f32x4*)(CS + 16 + 4 * g4); }
  wg_barrier();
  STAMP(30 * l + 17);
  PIN(W0[0]); PIN(W0[1]); if (two) { PIN(W1[0]); PIN(W1[1]); }
  PIN(cs0); PIN(cs1);
  const unsigned xsrc = OFF_XT + (2 * l + 1) * XT_BYTES + g * TILE_BYTES;
  f32x4 acc[2][4] = {};
  float ss = 0, qq = 0;
  const int st_t = w & 3, st_p = w >> 2;
  if (isT) { if (two) qb_loop<true, true>(c, 61, xsrc, wo, W0, W1, acc, st_t, st_p, ss, qq); else qb_loop<true, false>(c, 61, xsrc, wo, W0, W1, acc, st_t, st_p, ss, qq); }
  else { if (two) qb_loop<false, true>(c, 61, xsrc, wo, W0, W1, acc, st_t, st_p, ss, qq); else qb_loop<false, false>(c, 61, xsrc, wo, W0, W1, acc, st_t, st_p, ss, qq); }
  STAMP(30 * l + 19);
  ss = xrow16_sum(ss); qq = xrow16_sum(qq);
  if (g4 == 0) *(LAS f32x2*)((LAS char*)smem + L_STATS + (st_p * 64 + st_t * 16 + r16) * 8) = f32x2{ss, qq};
  finalize_stats<2>(c);
  STAMP(30 * l + 20);
  const unsigned KV = OFF_KVB + l * KVB_BYTES;
  LAS char* qs = (LAS char*)smem + L_QST;
  if (!isT) {
    f32x2 st[4];
#pragma unroll
    for (int t = 0; t < 4; t++) st[t] = tok_stat(c, 16 * t + r16);
    if (two) {
#pragma unroll
      for (int i = 0; i < 2; i++) {
        const int tile = t0 + i; const bool isq = tile < 4; const int hl = tile & 3;
        const f32x4 cs = i ? cs1 : cs0; const float sc = isq ? QSCALE : 1.0f;
#pragma unroll
        for (int u = 0; u < 2; u++) {
          const f32x4 c0 = acc[i][2 * u], c1 = acc[i][2 * u + 1];
          const float m0 = st[2 * u][0], m1 = st[2 * u + 1][0], r0 = st[2 * u][1] * sc, r1 = st[2 * u + 1][1] * sc;
          const unsigned a0 = cvtpk((c0[0] - m0 * cs[0]) * r0, (c0[1] - m0 * cs[1]) * r0), a1 = cvtpk((c0[2] - m0 * cs[2]) * r0, (c0[3] - m0 * cs[3]) * r0);
          const unsigned b0 = cvtpk((c1[0] - m1 * cs[0]) * r1, (c1[1] - m1 * cs[1]) * r1), b1 = cvtpk((c1[2] - m1 * cs[2]) * r1, (c1[3] - m1 * cs[3]) * r1);
          const auto x = __builtin_amdgcn_permlane16_swap(a0, b0, false, false), y = __builtin_amdgcn_permlane16_swap(a1, b1, false, false);
          const u32x4 v = {x[0], y[0], x[1], y[1]};
          const int tl = 16 * (g4 & 1) + r16;
          if (isq) *(LAS u32x4*)(qs + (hl * 64 + 32 * u + tl) * 32 + (g4 >> 1) * 16) = v;
          else st16c(c.rs, KV + ((4 * m + hl) * 64 + 2 * g + u) * REC + ((g4 >> 1) * 32 + tl) * 16, v);
        }
      }
    } else {
      const bool isq = g4 < 2; const float sc = isq ? QSCALE : 1.0f; const int hp = 2 * (g4 & 1);
#pragma unroll
      for (int t = 0; t < 4; t++) {
        const f32x4 cc = acc[0][t]; const float mm = st[t][0], rr = st[t][1] * sc;
        const unsigned a0 = cvtpk((cc[0] - mm * cs0[0]) * rr, (cc[1] - mm * cs0[1]) * rr), a1 = cvtpk((cc[2] - mm * cs0[2]) * rr, (cc[3] - mm * cs0[3]) * rr);
        if (isq) {
          *(LAS unsigned*)(qs + 8192 + (hp * 64 + 16 * t + r16) * 4) = a0;
          *(LAS unsigned*)(qs + 8192 + ((hp + 1) * 64 + 16 * t + r16) * 4) = a1;
        } else {
          const int T = 2 * g + (t >> 1), key = 16 * (t & 1) + r16;
          st4c(c.rs, KV + ((4 * m + hp) * 64 + T) * REC + REC_K1 + key * 4, a0);
          st4c(c.rs, KV + ((4 * m + hp + 1) * 64 + T) * REC + REC_K1 + key * 4, a1);
        }
      }
    }
  } else {
    const int nt = two ? 2 : 1;
    f32x4 sA[4], sB[4];
#pragma unroll
    for (int t = 0; t < 4; t++) { const LAS f32x4* sp = (const LAS f32x4*)((const LAS char*)smem + L_STAB + (16 * t + 4 * g4) * 8); sA[t] = sp[0]; sB[t] = sp[1]; }
#pragma unroll
    for (int i = 0; i < 2; i++) {
      if (i < nt) {
        const float cs = i ? cs1[0] : cs0[0];
#pragma unroll
        for (int u = 0; u < 2; u++) {
          unsigned pk[2][2];
#pragma unroll
          for (int tt = 0; tt < 2; tt++) {
            const int t = 2 * u + tt; const f32x4 a = acc[i][t];
            pk[tt][0] = cvtpk((a[0] - sA[t][0] * cs) * sA[t][1], (a[1] - sA[t][2] * cs) * sA[t][3]);
            pk[tt][1] = cvtpk((a[2] - sB[t][0] * cs) * sB[t][1], (a[3] - sB[t][2] * cs) * sB[t][3]);
          }
          const auto x = __builtin_amdgcn_permlane32_swap(pk[0][0], pk[1][0], false, false), y = __builtin_amdgcn_permlane32_swap(pk[0][1], pk[1][1], false, false);
          const u32x4 v = {x[0], y[0], x[1], y[1]};
          if (two) st16c(c.rs, KV + ((4 * m + t0 - 9 + i) * 64 + 2 * g + u) * REC + REC_V + (g4 * 18 + r16) * 16, v);
          else if (r16 < 8) st16c(c.rs, KV + ((4 * m + (r16 >> 1)) * 64 + 2 * g + u) * REC + REC_V + (g4 * 18 + 16 + (r16 & 1)) * 16, v);
        }
      }
    }
  }
  publish(c, F_KB + l);
  STAMP(30 * l + 21);
  const int r = lane & 31, h = lane >> 5, hl = w & 3, kh = w >> 2;
  const int li = 2 * l + 1, head = 4 * m + hl;
  bf16x8 q0[2]; unsigned qt[2];
#pragma unroll
  for (int a = 0; a < 2; a++) q_from_stage(c, hl, 32 * a + r, q0[a], qt[a]);
  if (w == 7) { const int gg = g - (lane & 7); poll1(c, flag_off(F_KB + l, gg < 0 ? 0 : gg, m), lane < 5 && gg >= 0); }
  wg_barrier();
  STAMP(30 * l + 22);
  const unsigned KH = KV + head * 64 * REC;
  const unsigned ok0 = lane * 16, ok1 = REC_K1 + r * 4;
  const bool vreal = r < 18;
  const unsigned ovn = REC_V + h * 288 + r * 16, ovc = OFF_CONST + v_const_off(r, h);
  const int Tb = 2 * g - 8 + 5 * kh;
  bf16x8 K0[5], V0[5], V1[5]; unsigned K1[5];
  auto ldkv = [&](int j) {
    const int T = Tb + j; const unsigned rp = KH + (T < 0 ? 0 : T) * REC;
    K0[j] = as_bf8(ld16c(c.rs, rp + ok0)); K1[j] = ld4c(c.rs, rp + ok1);
    const unsigned vo = vreal ? rp + ovn : ovc;
    V0[j] = as_bf8(ld16c(c.rs, vo)); V1[j] = as_bf8(ld16c(c.rs, vo + 576));
  };
  ldkv(4); ldkv(3);
  auto wpiece = [&](int i) { const int fr = i < 10 ? i * 8 + w : 80;
    glds16<false>(p.ws, OFF_WOB + m * WSL + fr * FRAG + lane * 16, c.lds0 + L_W + fr * FRAG); };
  const f32x16 zero = {};
  f32x16 mdiag, mfirst;
#pragma unroll
  for (int i = 0; i < 16; i++) { const int cc = (i & 3) + 8 * (i >> 2) + 4 * h; mdiag[i] = cc <= r ? 0.f : -1e30f; mfirst[i] = cc > r ? 0.f : -1e30f; }
#define B_TILE(a, j, Q1, BODY) do { \
    const bool use_ = (Tb + (j) >= 0) && (kh == 0 ? !((a) == 1 && (j) == 0) : !((a) == 0 && (j) == 4)); \
    if (use_) { \
      const bool fi_ = kh == 0 && (j) == (a), di_ = kh == 1 && (j) == 3 + (a); \
      f32x16 s = fi_ ? MFMA32K8(mk_k1(K1[j]), Q1, mfirst) : (di_ ? MFMA32K8(mk_k1(K1[j]), Q1, mdiag) : MFMA32K8(mk_k1(K1[j]), Q1, zero)); \
      s = MFMA32(K0[j], q0[a], s); \
      BODY \
    } } while (0)
  const bool live = Tb + 4 >= 0;
  float mref[2] = {-3.0e38f, -3.0e38f};
  if (live) {
    { const bf16x4 z = mk_q1(qt[0], 0.f, h); if (kh) B_TILE(0, 3, z, mref[0] = bf_round(tile_max(s));); else B_TILE(0, 4, z, mref[0] = bf_round(tile_max(s));); }
    { const bf16x4 z = mk_q1(qt[1], 0.f, h); B_TILE(1, 4, z, mref[1] = bf_round(tile_max(s));); }
  }
  ldkv(2); ldkv(1); ldkv(0);
  STAMP(30 * l + 23);
  f32x16 oa = zero, ob = zero;
  if (live) {
    for (int attempt = 0;; attempt++) {
      const bf16x4 q1a = mk_q1(qt[0], -mref[0], h), q1b = mk_q1(qt[1], -mref[1], h);
      oa = zero; ob = zero;
#define B_PV(O, j) u32x4 p0, p1; X4(s, 0); X4(s, 4); X4(s, 8); X4(s, 12); C2(s, 0, p0, 0); C2(s, 4, p0, 2); C2(s, 8, p1, 0); C2(s, 12, p1, 2); \
      O = MFMA32(V0[j], as_bf8(p0), O); O = MFMA32(V1[j], as_bf8(p1), O);
      B_TILE(0, 4, q1a, B_PV(oa, 4)); B_TILE(1, 4, q1b, B_PV(ob, 4));
      if (attempt == 0) { wpiece(0); wpiece(1); wpiece(2); }
      B_TILE(0, 3, q1a, B_PV(oa, 3)); B_TILE(1, 3, q1b, B_PV(ob, 3));
      if (attempt == 0) { wpiece(3); wpiece(4); wpiece(5); }
      B_TILE(0, 2, q1a, B_PV(oa, 2)); B_TILE(1, 2, q1b, B_PV(ob, 2));
      if (attempt == 0) { wpiece(6); wpiece(7); wpiece(8); }
      B_TILE(0, 1, q1a, B_PV(oa, 1)); B_TILE(1, 1, q1b, B_PV(ob, 1));
      if (attempt == 0) { wpiece(9); wpiece(10); }
      B_TILE(0, 0, q1a, B_PV(oa, 0)); B_TILE(1, 0, q1b, B_PV(ob, 0));
#undef B_PV
      const bool bad = (h == 0) && (!(oa[10] < L_LIMIT) || !(ob[10] < L_LIMIT));
      if (attempt || !__any(bad)) break;
      float ma = -3.0e38f, mb = -3.0e38f;
      const bf16x4 za = mk_q1(qt[0], 0.f, h), zb = mk_q1(qt[1], 0.f, h);
      B_TILE(0, 0, za, ma = fmaxf(ma, tile_max(s));); B_TILE(0, 1, za, ma = fmaxf(ma, tile_max(s));); B_TILE(0, 2, za, ma = fmaxf(ma, tile_max(s)););
      B_TILE(0, 3, za, ma = fmaxf(ma, tile_max(s));); B_TILE(0, 4, za, ma = fmaxf(ma, tile_max(s)););
      B_TILE(1, 0, zb, mb = fmaxf(mb, tile_max(s));); B_TILE(1, 1, zb, mb = fmaxf(mb, tile_max(s));); B_TILE(1, 2, zb, mb = fmaxf(mb, tile_max(s)););
      B_TILE(1, 3, zb, mb = fmaxf(mb, tile_max(s));); B_TILE(1, 4, zb, mb = fmaxf(mb, tile_max(s)););
      mref[0] = bf_round(ma); mref[1] = bf_round(mb);
    }
  } else {
#pragma unroll
    for (int i = 0; i < 11; i++) wpiece(i);
  }
#undef B_TILE
  STAMP(30 * l + 24);
  combine_store(c, oa, ob, mref[0], mref[1], OFF_OT + li * XT_BYTES);
  publish(c, F_O + li);
  STAMP(30 * l + 25);
}

__global__ void __launch_bounds__(512, 2) k_main(Params p, int ub, int ue) {
  extern __shared__ __attribute__((aligned(16))) char smem[];
  Ctx c;
  c.p = p; c.smem = smem; c.lds0 = (unsigned)(uintptr_t)smem;
  c.rs = __builtin_amdgcn_make_buffer_rsrc(p.ws, 0, 0x10000000, 0x00020000);
  { const int b = blockIdx.x, xcd = b & 7, idx = b >> 3; c.g = xcd * 4 + (idx >> 3); c.m = idx & 7; }
  c.lane = threadIdx.x & 63; c.w = __builtin_amdgcn_readfirstlane(threadIdx.x >> 6);
  c.r16 = c.lane & 15; c.g4 = c.lane >> 4;
  STAMP(0);
  for (int l = 0; l < 2; l++) {
    const int u = 4 * l;
    bf16x8 W[KSTEPS];
    unit_a(c, l, l == 0, W);
    unit_out<2, false, false>(c, 2 * l, 0, W);
    unit_b(c, l);
    unit_out<1, true, true>(c, 2 * l + 1, OFF_WQA + c.m * WSL, W);
    STAMP(30 * l + 30);
  }
}

extern "C" void kernel_launch(void* const* d_in, const int* in_sizes, int n_in, void* d_out, int out_size, void* d_ws, size_t ws_size,
                              hipStream_t stream) {
  Params p;
  p.x0 = (const unsigned short*)d_in[0]; p.wq = (const unsigned short*)d_in[1]; p.wk = (const unsigned short*)d_in[2];
  p.wv = (const unsigned short*)d_in[3]; p.woA = (const unsigned short*)d_in[4];
  p.wqkv = (const unsigned short*)d_in[6]; p.woB = (const unsigned short*)d_in[7];
  p.ws = (char*)d_ws; p.out = (unsigned short*)d_out;
  (void)hipFuncSetAttribute((const void*)k_main, hipFuncAttributeMaxDynamicSharedMemorySize, LDS_TOTAL);
  k_setup<<<SETUP_JOBS, 512, 0, stream>>>(p);
  k_main<<<256, 512, LDS_TOTAL, stream>>>(p, 0, 8);
}
```

```cpp
#include <hip/hip_runtime.h>
#include <hip/hip_bf16.h>
#include <stdint.h>
#define TL_LATE
#define CODE_PREFETCH 98304
#include <type_traits>

typedef short bf16x8 __attribute__((ext_vector_type(8)));
typedef short bf16x4 __attribute__((ext_vector_type(4)));
typedef __bf16 bf2_t __attribute__((ext_vector_type(2)));
typedef float f32x2 __attribute__((ext_vector_type(2)));
typedef float f32x4 __attribute__((ext_vector_type(4)));
typedef float f32x16 __attribute__((ext_vector_type(16)));
typedef unsigned u32x2 __attribute__((ext_vector_type(2)));
typedef unsigned u32x4 __attribute__((ext_vector_type(4)));
typedef unsigned u32x4a __attribute__((ext_vector_type(4), aligned(4)));
#define LAS __attribute__((address_space(3)))
#define GAS __attribute__((address_space(1)))
#define DI __device__ __forceinline__
#define MFMA16x16(a, b, c) __builtin_amdgcn_mfma_f32_16x16x32_bf16(a, b, c, 0, 0, 0)
#define MFMA32(a, b, c) __builtin_amdgcn_mfma_f32_32x32x16_bf16(a, b, c, 0, 0, 0)
#define MFMA32K8(a, b, c) __builtin_amdgcn_mfma_f32_32x32x8bf16_1k(a, b, c, 0, 0, 0)
#define SBAR() __builtin_amdgcn_sched_barrier(0)
#define PIN(x) asm volatile("" : "+v"(x))

constexpr int SEQ = 2048, DM = 576;
constexpr int KSTEPS = 18;
constexpr int FRAG = 1024;
constexpr int TILE_BYTES = 4 * KSTEPS * FRAG;
constexpr int WSL = 4 * KSTEPS * FRAG + KSTEPS * 512;
constexpr int REC = 2304;
constexpr int REC_K1 = 1024, REC_V = 1152;
constexpr float QSCALE = 0.23570226039551584f * 1.4426950408889634f;
static_assert(__builtin_bit_cast(unsigned, QSCALE) == 0x3eae1a92u, "QSCALE bits");
constexpr float LN_EPS = 1e-5f;
constexpr float L_LIMIT = 1.8446744e19f;

constexpr unsigned XT_BYTES = SEQ * DM * 2;
constexpr unsigned NBUF = 1, STEP_BUF = 0;
constexpr unsigned OFF_XT = 0;
constexpr unsigned OFF_OT = OFF_XT + NBUF * XT_BYTES;
constexpr unsigned KVA_BYTES = 64 * REC;
constexpr unsigned OFF_KVA = OFF_OT + NBUF * XT_BYTES;
constexpr unsigned KVB_BYTES = 32 * 64 * REC;
constexpr unsigned OFF_KVB = OFF_KVA + 2 * KVA_BYTES;
constexpr unsigned OFF_WQA = OFF_KVB + (STEP_BUF ? 2 : 1) * KVB_BYTES;
constexpr unsigned OFF_WOA = OFF_WQA + 8 * WSL;
constexpr unsigned OFF_WOB = OFF_WOA + 8 * WSL;
constexpr unsigned OFF_WKV = OFF_WOB + 8 * WSL;
constexpr unsigned OFF_WB = OFF_WKV + 4 * KSTEPS * FRAG;
constexpr unsigned OFF_CSP = OFF_WB + 8 * 14 * KSTEPS * FRAG;
constexpr unsigned OFF_XST = OFF_CSP + 8 * KSTEPS * 896;
constexpr unsigned OFF_FLAGS = OFF_XST + 32 * 8 * 64 * 8;
constexpr int NFL = 12, F_X = 0, F_O = 3, F_KV = 7, F_KB = 9, F_SET = 11;
constexpr unsigned OFF_EPOCH = OFF_FLAGS + NFL * 32 * 128;
constexpr unsigned WS_END = OFF_EPOCH + 128;
constexpr unsigned WS_SPAN = 64u << 20;
static_assert(WS_END + (4u << 20) <= WS_SPAN, "workspace span");
constexpr unsigned long long TAG_STEP = 0x9E3779B97F4A7C15ull;

constexpr int L_A = 0;
constexpr int L_W = TILE_BYTES;
constexpr int L_CONST = KVA_BYTES;
constexpr int L_QST = L_CONST + 1024;
constexpr int L_QSTB = 32768;
constexpr int L_STATS = L_QST + 9216;
constexpr int L_CSB = L_STATS + 3072;
constexpr int L_JUNK = L_CSB + 1024;
constexpr int LDS_TOTAL = 163840;
static_assert(L_STATS >= L_W + WSL + 1024 && L_JUNK + 256 <= 163840 && WSL <= KVA_BYTES, "LDS map");

#define STAMP(i) do {} while (0)

struct Params {
  const unsigned short* x0;  const unsigned short* wq; const unsigned short* wk; const unsigned short* wv; const unsigned short* woA;
  const unsigned short* wqkv; const unsigned short* woB;
  char* ws; unsigned short* out;
};

typedef __amdgpu_buffer_rsrc_t rsrc_t;
DI unsigned cvtpk(float a, float b) { f32x2 v = {a, b}; bf2_t r = __builtin_convertvector(v, bf2_t); return __builtin_bit_cast(unsigned, r); }
DI float bflo(unsigned u) { return __uint_as_float(u << 16); }
DI float bfhi(unsigned u) { return __uint_as_float(u & 0xffff0000u); }
DI float dot2(unsigned a, unsigned b, float c) { return __builtin_amdgcn_fdot2_f32_bf16(__builtin_bit_cast(bf2_t, a), __builtin_bit_cast(bf2_t, b), c, false); }
DI float xrow16_sum(float x) {
  auto s = __builtin_amdgcn_permlane16_swap(__float_as_uint(x), __float_as_uint(x), false, false);
  x = __uint_as_float(s[0]) + __uint_as_float(s[1]);
  auto t = __builtin_amdgcn_permlane32_swap(__float_as_uint(x), __float_as_uint(x), false, false);
  return __uint_as_float(t[0]) + __uint_as_float(t[1]);
}
DI float xhalf_max(float x) {
  auto t = __builtin_amdgcn_permlane32_swap(__float_as_uint(x), __float_as_uint(x), false, false);
  return fmaxf(__uint_as_float(t[0]), __uint_as_float(t[1]));
}
DI float lo_half(float x) {
  auto t = __builtin_amdgcn_permlane32_swap(__float_as_uint(x), __float_as_uint(x), false, false);
  return __uint_as_float(t[0]);
}
template <bool SC1> DI void glds16(const char* base, unsigned voff, unsigned lds_base) {
  unsigned sv;
  lds_base = __builtin_amdgcn_readfirstlane(lds_base);
  if (SC1) asm volatile("s_mov_b32 %0, m0\n\ts_mov_b32 m0, %3\n\ts_nop 0\n\tglobal_load_lds_dwordx4 %1, %2 sc1\n\ts_mov_b32 m0, %0"
                        : "=&s"(sv) : "v"(voff), "s"(base), "s"(lds_base) : "memory");
  else     asm volatile("s_mov_b32 %0, m0\n\ts_mov_b32 m0, %3\n\ts_nop 0\n\tglobal_load_lds_dwordx4 %1, %2\n\ts_mov_b32 m0, %0"
                        : "=&s"(sv) : "v"(voff), "s"(base), "s"(lds_base) : "memory");
}
DI void glds4(const char* base, unsigned voff, unsigned lds_base) {
  unsigned sv;
  lds_base = __builtin_amdgcn_readfirstlane(lds_base);
  asm volatile("s_mov_b32 %0, m0\n\ts_mov_b32 m0, %3\n\ts_nop 0\n\tglobal_load_lds_dword %1, %2\n\ts_mov_b32 m0, %0"
               : "=&s"(sv) : "v"(voff), "s"(base), "s"(lds_base) : "memory");
}
typedef short s16x4 __attribute__((ext_vector_type(4)));
DI bf16x8 tr_frag(const LAS char* p0, const LAS char* p1) {
  const s16x4 a = __builtin_amdgcn_ds_read_tr16_b64_v4i16((LAS s16x4*)p0), b = __builtin_amdgcn_ds_read_tr16_b64_v4i16((LAS s16x4*)p1);
  return __builtin_shufflevector(a, b, 0, 1, 2, 3, 4, 5, 6, 7);
}
DI void wait_vm0() { asm volatile("s_waitcnt vmcnt(0)" ::: "memory"); }
DI void wg_barrier() { asm volatile("s_waitcnt lgkmcnt(0)\n\ts_barrier" ::: "memory"); }
DI bf16x8 ld_frag_l(const LAS char* p) { return *(const LAS bf16x8*)p; }
DI u32x4 as_u4(bf16x8 v) { return __builtin_bit_cast(u32x4, v); }
DI bf16x8 as_bf8(u32x4 v) { return __builtin_bit_cast(bf16x8, v); }
DI unsigned ozero() { unsigned z; asm volatile("v_mov_b32 %0, 0" : "=v"(z)); return z; }
DI u32x4 ozero4() { const unsigned z = ozero(); return u32x4{z, z, z, z}; }
DI f32x16 ozero16() { const float z = __uint_as_float(ozero()); f32x16 v; for (int i = 0; i < 16; i++) v[i] = z; return v; }
template <unsigned V> DI unsigned oconst() { unsigned z; asm volatile("v_mov_b32 %0, %1" : "=v"(z) : "i"(V)); return z; }
DI bf16x8 ones8() { const unsigned v = oconst<0x3f803f80u>(); return as_bf8(u32x4{v, v, v, v}); }
constexpr bool WSC = true;
constexpr bool WBSC = true;
DI u32x4 ld16b(rsrc_t r, unsigned off) { return __builtin_amdgcn_raw_buffer_load_b128(r, off, 0, WBSC ? 16 : 0); }
DI u32x4 ld16w(rsrc_t r, unsigned off) { return __builtin_amdgcn_raw_buffer_load_b128(r, off, 0, WSC ? 16 : 0); }
DI u32x4 ld16c(rsrc_t r, unsigned off) { return __builtin_amdgcn_raw_buffer_load_b128(r, off, 0, 16); }
DI u32x2 ld8c(rsrc_t r, unsigned off) { return __builtin_amdgcn_raw_buffer_load_b64(r, off, 0, 16); }
DI unsigned ld4c(rsrc_t r, unsigned off) { return __builtin_amdgcn_raw_buffer_load_b32(r, off, 0, 16); }
DI void st16c(rsrc_t r, unsigned off, u32x4 v) { __builtin_amdgcn_raw_buffer_store_b128(v, r, off, 0, 16); }
DI void st8c(rsrc_t r, unsigned off, u32x2 v) { __builtin_amdgcn_raw_buffer_store_b64(v, r, off, 0, 16); }
DI void st4c(rsrc_t r, unsigned off, unsigned v) { __builtin_amdgcn_raw_buffer_store_b32(v, r, off, 0, 16); }

DI int operm(int ks, int g4, int j) {
  const int c = 4 * ks + g4;
  return c < 64 ? 18 * (c >> 1) + 8 * (j >> 2) + 4 * (c & 1) + (j & 3) : 18 * (4 * (c - 64) + (j >> 1)) + 16 + (j & 1);
}

struct Ctx {
  Params p; rsrc_t rs; char* smem; unsigned lds0; unsigned long long tag;
  int b, g, m, lane, w, r16, g4; unsigned fb;
};
DI Ctx fresh(const Ctx& c0) {
  Ctx c = c0;
  asm volatile("v_mbcnt_lo_u32_b32 %0, -1, 0\n\tv_mbcnt_hi_u32_b32 %0, -1, %0" : "=v"(c.lane));
  c.fb = OFF_FLAGS;
  asm volatile("" : "+s"(c.w), "+s"(c.g), "+s"(c.m), "+s"(c.lds0), "+s"(c.b), "+s"(c.tag), "+s"(c.fb));
  c.r16 = c.lane & 15; c.g4 = c.lane >> 4;
  return c;
}
typedef unsigned long long u64;
DI unsigned flag_off(const Ctx& c, int id, int g, int word) { return c.fb + ((id * 32 + g) * 16 + word) * 8; }
DI void flag_set(const Ctx& c, int id, int word) { __hip_atomic_store((GAS u64*)(c.p.ws + flag_off(c, id, c.g, word)), c.tag, __ATOMIC_RELAXED, __HIP_MEMORY_SCOPE_AGENT); }
DI u64 flag_ld(const Ctx& c, unsigned off) { return __hip_atomic_load((GAS u64*)(c.p.ws + off), __ATOMIC_RELAXED, __HIP_MEMORY_SCOPE_AGENT); }
template <int N> DI void poll(const Ctx& c, const unsigned (&off)[N], bool need) {
#pragma nounroll
  for (unsigned spins = 0; spins < (1u << 17); spins++) {
    u64 v[N]; bool ok = true;
#pragma unroll
    for (int i = 0; i < N; i++) v[i] = need ? flag_ld(c, off[i]) : c.tag;
#pragma unroll
    for (int i = 0; i < N; i++) ok &= v[i] == c.tag;
    if (__all(ok)) break;
    __builtin_amdgcn_s_sleep(1);
  }
}
DI void poll1(const Ctx& c, unsigned off, bool need) {
  const unsigned o = need ? off : c.fb;
  const u64 bal = __ballot(need);
  const u64 nm = (u64)(unsigned)__builtin_amdgcn_readfirstlane((int)(unsigned)bal) | ((u64)(unsigned)__builtin_amdgcn_readfirstlane((int)(unsigned)(bal >> 32)) << 32);
  u64 a, b, d, t; unsigned n;
  asm volatile(
      "s_mov_b32 %4, 0x8000\n"
      "1:\n\t"
      "global_load_dwordx2 %0, %5, %6 sc1\n\ts_sleep 1\n\t"
      "global_load_dwordx2 %1, %5, %6 sc1\n\ts_sleep 1\n\t"
      "global_load_dwordx2 %2, %5, %6 sc1\n\t"
      "s_waitcnt vmcnt(2)\n\tv_cmp_eq_u64_e32 vcc, %7, %0\n\ts_nop 4\n\ts_andn2_b64 %3, %8, vcc\n\ts_cbranch_scc0 2f\n\t"
      "s_waitcnt vmcnt(1)\n\tv_cmp_eq_u64_e32 vcc, %7, %1\n\ts_nop 4\n\ts_andn2_b64 %3, %8, vcc\n\ts_cbranch_scc0 2f\n\t"
      "s_waitcnt vmcnt(0)\n\tv_cmp_eq_u64_e32 vcc, %7, %2\n\ts_nop 4\n\ts_andn2_b64 %3, %8, vcc\n\ts_cbranch_scc0 2f\n\t"
      "s_sub_u32 %4, %4, 1\n\ts_cmp_lg_u32 %4, 0\n\ts_cbranch_scc1 1b\n"
      "2:\n\ts_waitcnt vmcnt(0)"
      : "=&v"(a), "=&v"(b), "=&v"(d), "=&s"(t), "=&s"(n) : "v"(o), "s"(c.p.ws), "s"(c.tag), "s"(nm) : "vcc", "scc", "memory");
}
DI void publish(const Ctx& c, int id) { wait_vm0(); wg_barrier(); if (c.w == 0 && c.lane == 0) flag_set(c, id, c.m); }

struct TUnit { unsigned dst, csp; int kind, zero; };
constexpr int C_WQ = 0, C_WOA = 576, C_WOB = C_WOA + 720, C_WB = C_WOB + 720, C_WKV = C_WB + 8 * 12 * KSTEPS, C_END = C_WKV + 2 * KSTEPS;
constexpr int G_WQ = 0, G_WB = 144, G_WKV = G_WB + 288, G_END = G_WKV + 2 * KSTEPS;
DI u32x4 tile_load_c(const Ctx& c, int u) {
  const int rho = c.lane >> 1, half = c.lane & 1;
  const char* src = (const char*)c.p.wq; int row = rho, col = 8 * half, ld = DM;
  if (u < C_WOA) { const int m = u / 72, ft = (u / KSTEPS) & 3, ks = u % KSTEPS; row = 32 * ks + rho; col = 18 * (4 * m + ft) + 8 * half; }
  else if (u < C_WB) { const bool isB = u >= C_WOB; const int v = u - (isB ? C_WOB : C_WOA), m = v / 90, ft = (v / KSTEPS) % 5, ks = v % KSTEPS;
    src = (const char*)(isB ? c.p.woB : c.p.woA); row = operm(ks, rho >> 3, rho & 7);
    col = 72 * m + 16 * ft + (ft < 4 ? 8 * half : 0); }
  else if (u < C_WKV) { const int v = u - C_WB, m = v / (12 * KSTEPS), j = (v / KSTEPS) % 12, ks = v % KSTEPS;
    src = (const char*)c.p.wqkv; ld = 3 * DM; row = 32 * ks + rho; col = (j >> 2) * DM + 18 * (4 * m + (j & 3)) + 8 * half; }
  else if (u < C_END) { const int v = u - C_WKV; src = (const char*)(v >= KSTEPS ? c.p.wv : c.p.wk); ld = 18; row = 32 * (v % KSTEPS) + rho; }
  return *(const u32x4a*)(src + (row * ld + col) * 2);
}
DI TUnit tile_desc_c(int u) {
  TUnit t = {0u, 0u, 0, 0};
  if (u < C_WOA) { const int m = u / 72; t.dst = OFF_WQA + m * WSL + (u % 72) * FRAG; }
  else if (u < C_WB) { const bool isB = u >= C_WOB; const int v = u - (isB ? C_WOB : C_WOA), m = v / 90, e = v % 90;
    const unsigned base = (isB ? OFF_WOB : OFF_WOA) + m * WSL;
    if (e < 72) t.dst = base + e * FRAG; else { t.kind = 1; t.dst = base + 4 * KSTEPS * FRAG + (e - 72) * 512; } }
  else if (u < C_WKV) { const int v = u - C_WB, m = v / (12 * KSTEPS), j = (v / KSTEPS) % 12, ks = v % KSTEPS;
    const int tl = j < 8 ? j : j + 1;
    t.dst = OFF_WB + ((m * 14 + tl) * KSTEPS + ks) * FRAG; t.csp = OFF_CSP + (m * KSTEPS + ks) * 896 + tl * 64; }
  else if (u < C_END) { const int v = u - C_WKV; t.dst = OFF_WKV + (2 * (v / KSTEPS) * KSTEPS + v % KSTEPS) * FRAG; }
  else t.kind = -1;
  return t;
}
DI u32x4 tile_load_g(const Ctx& c, int u0) {
  const int rho = c.lane >> 1, half = c.lane & 1, u = u0 < G_END ? u0 : G_END - 1;
  const char* src; int row, ld, col0, cstep;
  if (u < G_WB) { const int m = u / KSTEPS; src = (const char*)c.p.wq; ld = DM; row = 32 * (u % KSTEPS) + rho; col0 = 72 * m + 16; cstep = 18; }
  else if (u < G_WKV) { const int v = u - G_WB, m = v / (2 * KSTEPS), vt = (v / KSTEPS) & 1;
    src = (const char*)c.p.wqkv; ld = 3 * DM; row = 32 * (v % KSTEPS) + rho; col0 = (vt ? 2 : half) * DM + 72 * m + 16; cstep = 18; }
  else { const int v = u - G_WKV; src = (const char*)(v >= KSTEPS ? c.p.wv : c.p.wk); ld = 18; row = 32 * (v % KSTEPS) + rho; col0 = 16; cstep = 0; }
  u32x4 v;
#pragma unroll
  for (int i = 0; i < 4; i++) v[i] = *(const unsigned*)(src + (row * ld + col0 + cstep * i) * 2);
  return v;
}
DI TUnit tile_desc_g(int u) {
  TUnit t = {0u, 0u, 0, 0};
  if (u < G_WB) { t.kind = 1; t.zero = 1; t.dst = OFF_WQA + (u / KSTEPS) * WSL + 4 * KSTEPS * FRAG + (u % KSTEPS) * 512; }
  else if (u < G_WKV) { const int v = u - G_WB, m = v / (2 * KSTEPS), vt = (v / KSTEPS) & 1; t.zero = vt; t.dst = OFF_WB + ((m * 14 + (vt ? 13 : 8)) * KSTEPS + v % KSTEPS) * FRAG;
    t.csp = OFF_CSP + (m * KSTEPS + v % KSTEPS) * 896 + (vt ? 13 : 8) * 64; }
  else if (u < G_END) { const int v = u - G_WKV; t.zero = 2; t.dst = OFF_WKV + ((2 * (v / KSTEPS) + 1) * KSTEPS + v % KSTEPS) * FRAG; }
  else t.kind = -1;
  return t;
}
DI void tile_store(const Ctx& c, const TUnit& t, u32x4 v, int scratch) {
  if (t.kind < 0) return;
  if (t.zero && (c.lane & 1)) v = u32x4{0, 0, 0, 0};
  if (t.zero == 2) { v[1] = 0; v[2] = 0; v[3] = 0; }
  LAS char* sc = (LAS char*)c.smem + scratch;
  *(LAS u32x4*)(sc + (c.lane >> 1) * 32 + (c.lane & 1) * 16) = v;
  const LAS char* rp = sc + (8 * c.g4 + (c.r16 >> 2)) * 32 + (c.lane & 3) * 8;
  const u32x4 f = as_u4(tr_frag(rp, rp + 128));
  if (t.kind == 1) { if (c.r16 < 8) st16c(c.rs, t.dst + (c.g4 * 8 + c.r16) * 16, f); }
  else st16c(c.rs, t.dst + c.lane * 16, f);
  if (t.csp) {
    float a = 0.f;
#pragma unroll
    for (int i = 0; i < 4; i++) a = dot2(f[i], 0x3f803f80u, a);
    a = xrow16_sum(a);
    if (c.g4 == 0) st4c(c.rs, t.csp + c.r16 * 4, __float_as_uint(a));
  }
}
DI void fill_csb(const Ctx& c) {
  const int t = c.w * 64 + c.lane;
  if (t < 224) {
    float v[KSTEPS], a = 0.f;
#pragma unroll
    for (int ks = 0; ks < KSTEPS; ks++) v[ks] = __uint_as_float(ld4c(c.rs, OFF_CSP + (c.m * KSTEPS + ks) * 896 + t * 4));
#pragma unroll
    for (int ks = 0; ks < KSTEPS; ks++) a += v[ks];
    *(LAS float*)((LAS char*)c.smem + L_CSB + t * 4) = a;
  }
}

template <bool CHUNKED> DI void dma_tile(const Ctx& c, unsigned src) {
#pragma unroll
  for (int i = 0; i < 9; i++) {
    const int pc = c.w + 8 * i, fr = (pc & 3) * KSTEPS + (pc >> 2);
    glds16<true>(c.p.ws, src + fr * FRAG + c.lane * 16, c.lds0 + L_A + fr * FRAG);
    if (CHUNKED && (i == 2 || i == 5)) __builtin_amdgcn_s_barrier();
  }
}
DI void dma_wslice(const Ctx& c, unsigned src) {
#pragma unroll
  for (int i = 0; i < 11; i++) {
    const int fr = i < 10 ? i * 8 + c.w : 80;
    glds16<WSC>(c.p.ws, src + fr * FRAG + c.lane * 16, c.lds0 + L_W + fr * FRAG);
  }
}
#define WAIT_VM(n) asm volatile("s_waitcnt vmcnt(" #n ")" ::: "memory")
DI void stats_acc(bf16x8 x, float& s, float& q) {
  const u32x4 u = as_u4(x);
#pragma unroll
  for (int i = 0; i < 4; i++) { s = dot2(u[i], 0x3f803f80u, s); q = dot2(u[i], u[i], q); }
}
DI f32x2 finish_stats(float s, float q) {
  const float mean = s * (1.0f / DM);
  const float var = fmaxf(q * (1.0f / DM) - mean * mean, 0.f);
  return f32x2{mean, __builtin_amdgcn_rsqf(var + LN_EPS)};
}
constexpr int L_STAB = L_STATS + 2048;
template <int NP> DI void finalize_stats(const Ctx& c) {
  wg_barrier();
  if (c.w == 0) {
    const LAS float* st = (const LAS float*)((LAS char*)c.smem + L_STATS);
    float s = 0.f, q = 0.f;
#pragma unroll
    for (int k = 0; k < NP; k++) { const f32x2 v = *(const LAS f32x2*)(st + (k * 64 + c.lane) * 2); s += v[0]; q += v[1]; }
    *(LAS f32x2*)((LAS char*)c.smem + L_STAB + c.lane * 8) = finish_stats(s, q);
  }
  wg_barrier();
}
struct XStat { u32x2 v[8]; };
DI XStat xstat_load(const Ctx& c) { XStat s;
#pragma unroll
  for (int mm = 0; mm < 8; mm++) s.v[mm] = ld8c(c.rs, OFF_XST + ((c.g * 8 + mm) * 64 + c.lane) * 8);
  return s; }
DI void xstat_finish(const Ctx& c, const XStat& x) {
  float s = 0.f, q = 0.f;
#pragma unroll
  for (int mm = 0; mm < 8; mm++) { s += __uint_as_float(x.v[mm][0]); q += __uint_as_float(x.v[mm][1]); }
  *(LAS f32x2*)((LAS char*)c.smem + L_STAB + c.lane * 8) = finish_stats(s, q);
}
DI f32x2 tok_stat(const Ctx& c, int tok) { return *(const LAS f32x2*)((const LAS char*)c.smem + L_STAB + tok * 8); }
DI void wmain_from_lds(const Ctx& c, bf16x8 (&W)[KSTEPS]) {
  const LAS char* wl = (const LAS char*)c.smem + L_W + ((c.w & 3) * KSTEPS) * FRAG + c.lane * 16;
#pragma unroll
  for (int ks = 0; ks < KSTEPS; ks++) W[ks] = ld_frag_l(wl + ks * FRAG);
}
DI void wtail_from_lds(const Ctx& c, bf16x8 (&WX)[KSTEPS], int lds_off) {
  const LAS char* wl = (const LAS char*)c.smem + lds_off + (c.g4 * 8 + (c.r16 & 7)) * 16;
#pragma unroll
  for (int ks = 0; ks < KSTEPS; ks++) WX[ks] = ld_frag_l(wl + ks * 512);
}
struct ATiled { const LAS char* b; DI bf16x8 operator()(int tt, int ks) const { return ld_frag_l(b + (tt * KSTEPS + ks) * FRAG); } };
struct ANat { const LAS char* b; int g4, fx; DI bf16x8 operator()(int tt, int ks) const { return ld_frag_l(b + tt * 16 * 1152 + (((4 * ks + g4) ^ fx) << 4)); } };
template <bool T, int D, class AF> DI f32x4 gemm1(const AF& A, int tt, const bf16x8 (&W)[KSTEPS]) {
  f32x4 acc = {}; bf16x8 x[KSTEPS];
#pragma unroll
  for (int ks = 0; ks < D; ks++) x[ks] = A(tt, ks);
  SBAR();
#pragma unroll
  for (int ks = 0; ks < KSTEPS; ks++) {
    if (ks + D < KSTEPS) x[ks + D] = A(tt, ks + D);
    acc = T ? MFMA16x16(x[ks], W[ks], acc) : MFMA16x16(W[ks], x[ks], acc);
    SBAR();
  }
  return acc;
}
template <int EX, bool CHUNK, class AF> DI void gemm2(const AF& A, int tt0, const bf16x8 (&W)[KSTEPS], const bf16x8 (&WX)[KSTEPS], f32x4& acc0, f32x4& acc1, f32x4& accx) {
  constexpr int D = 3;
  bf16x8 x0[KSTEPS], x1[KSTEPS];
  if (CHUNK) { WAIT_VM(6); wg_barrier(); }
#pragma unroll
  for (int ks = 0; ks < D; ks++) { x0[ks] = A(tt0, ks); x1[ks] = A(tt0 + 1, ks); }
  SBAR();
#pragma unroll
  for (int ks = 0; ks < KSTEPS; ks++) {
    if (CHUNK && ks + D == 6) { WAIT_VM(3); wg_barrier(); }
    if (CHUNK && ks + D == 12) { WAIT_VM(0); wg_barrier(); }
    if (ks + D < KSTEPS) { x0[ks + D] = A(tt0, ks + D); x1[ks + D] = A(tt0 + 1, ks + D); }
    acc0 = MFMA16x16(W[ks], x0[ks], acc0);
    acc1 = MFMA16x16(W[ks], x1[ks], acc1);
    if (EX == 1) accx = MFMA16x16(WX[ks], x0[ks], accx);
    if (EX == 2) accx = MFMA16x16(WX[ks], x1[ks], accx);
    SBAR();
  }
}

DI float tile_max(const f32x16& s) {
  float a = fmaxf(fmaxf(s[0], s[1]), s[2]), b = fmaxf(fmaxf(s[3], s[4]), s[5]);
#pragma unroll
  for (int i = 6; i < 16; i += 2) { a = fmaxf(a, s[i]); b = fmaxf(b, s[i + 1]); }
  return xhalf_max(fmaxf(a, b));
}
DI bf16x4 mk_q1(unsigned tail, float negm, int h) {
  const unsigned mb = cvtpk(negm, 0.f);
  u32x2 t = {h ? 0u : tail, h ? 0u : mb};
  return __builtin_bit_cast(bf16x4, t);
}
DI bf16x4 mk_k1(unsigned tail, unsigned one) { u32x2 t = {tail, one}; return __builtin_bit_cast(bf16x4, t); }
DI float bf_round(float x) { return bflo(cvtpk(x, 0.f)); }
DI int v_const_off(int r, int h) { return r == 18 ? (11 + 2 * h) * 16 : ((r >= 20 && r < 28) ? (12 + 2 * h) * 16 : (10 + 2 * h) * 16); }
#define MB() SBAR()
#define EX(x) __builtin_amdgcn_exp2f(x)
#define X4(S, o) do { S[o] = EX(S[o]); S[o + 1] = EX(S[o + 1]); S[o + 2] = EX(S[o + 2]); S[o + 3] = EX(S[o + 3]); } while (0)
#define XT(T, S) do { T[0] = EX(S[12]); T[1] = EX(S[13]); T[2] = EX(S[14]); T[3] = EX(S[15]); } while (0)
#define C2(S, o, P, pi) do { P[pi] = cvtpk(S[o], S[o + 1]); P[pi + 1] = cvtpk(S[o + 2], S[o + 3]); } while (0)

DI void q_from_stage(const Ctx& c, int base, int hl, int tok, bf16x8& q0, unsigned& qt) {
  const LAS char* qs = (const LAS char*)c.smem + base;
  q0 = ld_frag_l(qs + (hl * 64 + tok) * 32 + (c.lane >> 5) * 16);
  qt = *(const LAS unsigned*)(qs + 8192 + (hl * 64 + tok) * 4);
}
template <bool LDS_BUSY> DI void combine_store(const Ctx& c, const f32x16& oa, const f32x16& ob, float ma, float mb, unsigned OT) {
  const int lane = c.lane, w = c.w, kh = w >> 2, r = lane & 31, h = lane >> 5, head = 4 * c.m + (w & 3);
  if (LDS_BUSY) wg_barrier();
  { const f32x16& og = kh ? oa : ob;
    LAS float* d = (LAS float*)c.smem + w * 13 * 64 + lane;
#pragma unroll
    for (int i = 0; i < 12; i++) d[i * 64] = og[i];
    d[12 * 64] = kh ? ma : mb; }
  wg_barrier();
  f32x16 o = kh ? ob : oa; const float mo = kh ? mb : ma;
  const LAS float* s = (const LAS float*)c.smem + (w ^ 4) * 13 * 64 + lane;
  const float mp = s[12 * 64], mx = fmaxf(mo, mp), fo = EX(mo - mx), fp = EX(mp - mx);
#pragma unroll
  for (int i = 0; i < 12; i++) o[i] = o[i] * fo + s[i * 64] * fp;
  const float inv = __builtin_amdgcn_rcpf(lo_half(o[10]));
  const int tok = 64 * c.g + 32 * kh + r, tt = tok >> 4, rr = tok & 15;
  const u32x4 v = {cvtpk(o[0] * inv, o[1] * inv), cvtpk(o[2] * inv, o[3] * inv), cvtpk(o[4] * inv, o[5] * inv), cvtpk(o[6] * inv, o[7] * inv)};
  st16c(c.rs, OT + ((tt * 72 + 2 * head + h) * 16 + rr) * 16, v);
  if (h == 0) st4c(c.rs, OT + ((tt * 72 + 64 + c.m) * 16 + rr) * 16 + (head & 3) * 4, cvtpk(o[8] * inv, o[9] * inv));
}

template <bool NAT, bool WPRE> DI void unit_qa(const Ctx& c_in, int l) {
  const Ctx c = fresh(c_in);
  const Params& p = c.p;
  const int lane = c.lane, w = c.w, r16 = c.r16, g4 = c.g4, g = c.g, m = c.m, ft = w & 3, th = w >> 2;
  char* smem = c.smem;
  const int tkv = m >> 1;
  int exk = 0, te = 0;
  if (w == 0 || w == 1) { exk = 1; te = w; } else if (w == 6 || w == 7) { exk = 1; te = w - 4; }
  else if (th == (tkv >> 1)) { exk = (w & 1) ? 3 : 2; te = tkv; }
  const bool exT = exk >= 2 && (m & 1);
  const int kvt = (m & 1) * 2 + (exk - 2);
  bf16x8 W[KSTEPS], WX[KSTEPS];
  f32x4 cs = {}, csx = {};
  typename std::conditional<NAT, ANat, ATiled>::type A;
  if constexpr (NAT) A = ANat{(const LAS char*)smem + L_A + r16 * 1152, g4, (r16 >> 1) & 7}; else A = ATiled{(const LAS char*)smem + L_A + lane * 16};
  LAS char* qs = (LAS char*)smem + L_QST;
  auto colsum = [&] { const bf16x8 one = ones8();
#pragma unroll
    for (int ks = 0; ks < KSTEPS; ks++) cs = MFMA16x16(W[ks], one, cs);
  };
  auto colsumx = [&] { const bf16x8 one = ones8();
    if (exT) {
#pragma unroll
      for (int ks = 0; ks < KSTEPS; ks++) csx = MFMA16x16(one, WX[ks], csx);
    } else {
#pragma unroll
      for (int ks = 0; ks < KSTEPS; ks++) csx = MFMA16x16(WX[ks], one, csx);
    }
  };
  auto stats = [&] {
    bf16x8 xs[10];
#pragma unroll
    for (int i = 0; i < 5; i++) { const int ks = (i < 4 || ft < 2) ? 4 * i + ft : ft;
      xs[2 * i] = A(2 * th, ks); xs[2 * i + 1] = A(2 * th + 1, ks); }
    float s0 = 0, q0 = 0, s1 = 0, q1 = 0;
#pragma unroll
    for (int i = 0; i < 4; i++) { stats_acc(xs[2 * i], s0, q0); stats_acc(xs[2 * i + 1], s1, q1); }
    if (ft < 2) { stats_acc(xs[8], s0, q0); stats_acc(xs[9], s1, q1); }
    s0 = xrow16_sum(s0); q0 = xrow16_sum(q0); s1 = xrow16_sum(s1); q1 = xrow16_sum(q1);
    LAS float* st = (LAS float*)((LAS char*)smem + L_STATS);
    if (g4 == 0) {
      *(LAS f32x2*)(st + (ft * 64 + (2 * th) * 16 + r16) * 2) = f32x2{s0, q0};
      *(LAS f32x2*)(st + (ft * 64 + (2 * th + 1) * 16 + r16) * 2) = f32x2{s1, q1};
    }
  };
  auto kv_path = [&] {
    const f32x4 accx = exT ? gemm1<true, 6>(A, te, WX) : gemm1<false, 6>(A, te, WX);
    if (NAT) finalize_stats<4>(c);
    STAMP(30 * l + 3);
    const unsigned rec = OFF_KVA + l * KVA_BYTES + ((4 * g + te) >> 1) * REC; const int sh = te & 1;
    if (!exT) {
      const f32x2 s = tok_stat(c, 16 * te + r16); const float mean = s[0], rstd = s[1];
      const unsigned a0 = cvtpk((accx[0] - mean * csx[0]) * rstd, (accx[1] - mean * csx[1]) * rstd), a1 = cvtpk((accx[2] - mean * csx[2]) * rstd, (accx[3] - mean * csx[3]) * rstd);
      if (exk == 2) st8c(c.rs, rec + ((g4 >> 1) * 32 + 16 * sh + r16) * 16 + (g4 & 1) * 8, u32x2{a0, a1});
      else if (g4 == 0) st4c(c.rs, rec + REC_K1 + (16 * sh + r16) * 4, a0);
    } else {
      float v[4];
#pragma unroll
      for (int i = 0; i < 4; i++) { const f32x2 s = tok_stat(c, 16 * te + 4 * g4 + i); v[i] = (accx[i] - s[0] * csx[i]) * s[1]; }
      const int d = (exk == 2) ? r16 : 16 + r16;
      if (exk == 2 || r16 < 2) st8c(c.rs, rec + REC_V + ((sh * 2 + (g4 & 1)) * 18 + d) * 16 + (g4 >> 1) * 8, u32x2{cvtpk(v[0], v[1]), cvtpk(v[2], v[3])});
    }
    wait_vm0(); if (lane == 0) flag_set(c, F_KV + l, 2 * m + (exk - 2));
    STAMP(30 * l + 4);
  };
  if constexpr (NAT) {
    auto requests = [&] {
#pragma unroll
      for (int i = 0; i < 9; i++) {
        const int pc = w + 8 * i, P = 64 * pc + lane, row = P / 72, ch = (P % 72) ^ ((row >> 1) & 7);
        glds16<false>((const char*)p.x0, (64 * g + row) * 1152 + ch * 16, c.lds0 + L_A + pc * 1024);
      }
#pragma unroll
      for (int i = 0; i < 11; i++) {
        const int pc = i < 10 ? w + 8 * i : 80, P = 64 * pc + lane, row = P / 9, ch = P % 9;
        glds16<false>((const char*)p.wq, row * 1152 + 144 * m + ch * 16, c.lds0 + L_W + pc * 1024);
      }
    };
    auto build_w = [&] {
      const LAS char* wl = (const LAS char*)smem + L_W + (8 * g4 + (r16 >> 2)) * 144 + 8 * (lane & 3);
#pragma unroll
      for (int ks = 0; ks < KSTEPS; ks++) W[ks] = tr_frag(wl + ft * 32 + ks * 32 * 144, wl + ft * 32 + ks * 32 * 144 + 4 * 144);
      if (exk == 1) {
#pragma unroll
        for (int ks = 0; ks < KSTEPS; ks++) WX[ks] = tr_frag(wl + 128 + ks * 32 * 144, wl + 128 + ks * 32 * 144 + 4 * 144);
      }
    };
    auto tl_load = [&](u32x4 (&TV)[3]) { const int u = c.b * 8 + w; TV[0] = tile_load_c(c, u); TV[1] = tile_load_c(c, u + 2048); TV[2] = tile_load_g(c, u); };
    auto tl_store = [&](const u32x4 (&TV)[3]) { int u = c.b * 8 + w; asm volatile("" : "+s"(u)); const int sc = L_W + w * 1024;
      tile_store(c, tile_desc_c(u), TV[0], sc); tile_store(c, tile_desc_c(u + 2048), TV[1], sc); tile_store(c, tile_desc_g(u), TV[2], sc); };
    if (exk >= 2) {
      u32x4 rk[KSTEPS], TV[3];
      requests();
      const char* wkv = (const char*)((m & 1) ? p.wv : p.wk) + (lane >> 1) * 36 + (exk == 2 ? (lane & 1) * 16 : 20);
#pragma unroll
      for (int ks = 0; ks < KSTEPS; ks++) rk[ks] = *(const u32x4a*)(wkv + ks * 32 * 36);
      wait_vm0(); wg_barrier();
      STAMP(30 * l + 2);
      stats();
      const int q = r16 >> 2, pq = lane & 3;
      if (exk == 2) {
        LAS char* sc = (LAS char*)smem + L_W + WSL;
        const LAS char* rp = sc + (8 * g4 + q) * 32 + 8 * pq;
#pragma unroll
        for (int ks = 0; ks < KSTEPS; ks++) { *(LAS u32x4*)(sc + (lane >> 1) * 32 + (lane & 1) * 16) = rk[ks]; WX[ks] = tr_frag(rp, rp + 128); }
      } else {
        LAS char* sc = (LAS char*)smem + L_STATS + 2560;
        const LAS char* rp = sc + (8 * g4 + q) * 8 + 8 * pq;
#pragma unroll
        for (int ks = 0; ks < KSTEPS; ks++) { if (!(lane & 1)) *(LAS u32x2*)(sc + (lane >> 1) * 8) = u32x2{rk[ks][3], 0u}; WX[ks] = tr_frag(rp, rp + 32); }
      }
      colsumx();
      kv_path();
      tl_load(TV);
      build_w();
      wg_barrier();
      tl_store(TV);
    } else {
      u32x4 TV[3];
      requests();
      wait_vm0(); wg_barrier();
      STAMP(30 * l + 2);
      stats();
      tl_load(TV);
      build_w();
      finalize_stats<4>(c);
      wg_barrier();
      tl_store(TV);
      if (exk == 1) colsumx();
    }
    colsum();
  } else {
    if (!WPRE) { dma_wslice(c, OFF_WQA + m * WSL); wait_vm0(); wg_barrier(); }
    if (exk >= 2) {
#pragma unroll
      for (int ks = 0; ks < KSTEPS; ks++) WX[ks] = as_bf8(ld16w(c.rs, OFF_WKV + (kvt * KSTEPS + ks) * FRAG + lane * 16));
    }
    if (w == 7) poll1(c, flag_off(c, F_X + 2 * l - 1, g, lane & 7), lane < 8);
    wait_vm0();
    wg_barrier();
    STAMP(30 * l + 1);
    XStat xst; if (w == 6) xst = xstat_load(c);
    dma_tile<false>(c, OFF_XT + STEP_BUF * 2 * l * XT_BYTES + g * TILE_BYTES);
    wmain_from_lds(c, W);
    if (exk == 1) wtail_from_lds(c, WX, L_W + 4 * KSTEPS * FRAG);
    colsum(); if (exk) colsumx();
    wait_vm0();
    if (w == 6) xstat_finish(c, xst);
    wg_barrier();
    STAMP(30 * l + 2);
    if (exk >= 2) kv_path();
  }
  { f32x4 acc0 = {}, acc1 = {}, acct = {};
    if (exk != 1) gemm2<0, false>(A, 2 * th, W, W, acc0, acc1, acct);
    else if (te & 1) gemm2<2, false>(A, 2 * th, W, WX, acc0, acc1, acct);
    else gemm2<1, false>(A, 2 * th, W, WX, acc0, acc1, acct);
    const float qsc = __uint_as_float(oconst<0x3eae1a92u>());
    unsigned qtail0 = 0, qtail1 = 0;
    if (exk == 1) { const f32x2 s = tok_stat(c, 16 * te + r16); const float mean = s[0], rstd = s[1] * qsc;
      qtail0 = cvtpk((acct[0] - mean * csx[0]) * rstd, (acct[1] - mean * csx[1]) * rstd); qtail1 = cvtpk((acct[2] - mean * csx[2]) * rstd, (acct[3] - mean * csx[3]) * rstd); }
    const f32x2 s0 = tok_stat(c, (2 * th) * 16 + r16), s1 = tok_stat(c, (2 * th + 1) * 16 + r16);
    const float m0 = s0[0], r0 = s0[1] * qsc, m1 = s1[0], r1 = s1[1] * qsc;
    const unsigned a0 = cvtpk((acc0[0] - m0 * cs[0]) * r0, (acc0[1] - m0 * cs[1]) * r0), a1 = cvtpk((acc0[2] - m0 * cs[2]) * r0, (acc0[3] - m0 * cs[3]) * r0);
    const unsigned b0 = cvtpk((acc1[0] - m1 * cs[0]) * r1, (acc1[1] - m1 * cs[1]) * r1), b1 = cvtpk((acc1[2] - m1 * cs[2]) * r1, (acc1[3] - m1 * cs[3]) * r1);
    if (NAT) {
      auto put = [&](int F, int tok, unsigned v) { const int hh = F / 18, d = F % 18;
        *(LAS unsigned*)(qs + (d < 16 ? (hh * 64 + tok) * 32 + 2 * d : 8192 + (hh * 64 + tok) * 4)) = v; };
      const int F0 = 16 * ft + 4 * g4;
      put(F0, 32 * th + r16, a0); put(F0 + 2, 32 * th + r16, a1); put(F0, 32 * th + 16 + r16, b0); put(F0 + 2, 32 * th + 16 + r16, b1);
      if (exk == 1 && g4 < 2) { put(64 + 4 * g4, 16 * te + r16, qtail0); put(64 + 4 * g4 + 2, 16 * te + r16, qtail1); }
    } else {
      const auto x = __builtin_amdgcn_permlane16_swap(a0, b0, false, false), y = __builtin_amdgcn_permlane16_swap(a1, b1, false, false);
      *(LAS u32x4*)(qs + (ft * 64 + 16 * (2 * th + (g4 & 1)) + r16) * 32 + (g4 >> 1) * 16) = u32x4{x[0], y[0], x[1], y[1]};
      if (exk == 1 && g4 < 2) {
        *(LAS unsigned*)(qs + 8192 + ((2 * g4) * 64 + 16 * te + r16) * 4) = qtail0;
        *(LAS unsigned*)(qs + 8192 + ((2 * g4 + 1) * 64 + 16 * te + r16) * 4) = qtail1;
      }
    } }
  STAMP(30 * l + 5);
  if (w == 5) { const unsigned v = (lane == 11 || lane == 13 || lane == 47 || lane == 49) ? 0x3f803f80u : 0u; *(LAS u32x4*)((LAS char*)smem + L_CONST + lane * 16) = u32x4{v, v, v, v}; }
  const int c0 = (c.b & 3);
  const unsigned KV = OFF_KVA + l * KVA_BYTES;
  if (w == 0) {
    const unsigned o[2] = {flag_off(c, F_KV + l, 4 * c0 + g4, r16), flag_off(c, F_KV + l, 16 + 4 * c0 + g4, r16)};
    poll<2>(c, o, true);
  }
  wg_barrier();
  STAMP(30 * l + 6);
  auto dma_piece = [&](int j, int i) {
    int pc = w + 8 * i; if (pc >= 36) pc -= 4;
    const unsigned o = (pc >= 18 ? 32 * REC : 0) + ((c0 + j) & 3) * 8 * REC + (pc >= 18 ? pc - 18 : pc) * 1024;
    glds16<true>(p.ws, KV + o + lane * 16, c.lds0 + o);
  };
#pragma unroll
  for (int i = 0; i < 5; i++) dma_piece(0, i);
  wait_vm0();
  wg_barrier();
  if (NAT && w == 0 && lane == 0) flag_set(c, F_SET, m);
  STAMP(30 * l + 7);
}

DI void unit_attn_a(const Ctx& c_in, int l, bf16x8 (&WOUT)[KSTEPS]) {
  const Ctx c = fresh(c_in);
  const Params& p = c.p;
  const int lane = c.lane, w = c.w, r16 = c.r16, g4 = c.g4, m = c.m, ft = w & 3;
  char* smem = c.smem;
  const int r = lane & 31, h = lane >> 5, hl = w & 3, kh = w >> 2;
  const int c0 = (c.b & 3);
  const unsigned KV = OFF_KVA + l * KVA_BYTES;
  const unsigned kone = oconst<0x3f80u>();
  bf16x8 q0[2]; unsigned qt[2];
#pragma unroll
  for (int a = 0; a < 2; a++) q_from_stage(c, L_QST, hl, 32 * a + r, q0[a], qt[a]);
  auto dma2 = [&](int k) {
    if (w < 4) {
      if (k == 0) { unsigned o[6];
#pragma unroll
        for (int i = 0; i < 3; i++) { o[2 * i] = flag_off(c, F_KV + l, 4 * ((c0 + 1 + i) & 3) + g4, r16); o[2 * i + 1] = flag_off(c, F_KV + l, 16 + 4 * ((c0 + 1 + i) & 3) + g4, r16); }
        poll<6>(c, o, true); }
#pragma unroll
      for (int e = 0; e < 4; e++) {
        int q = w + 4 * (4 * k + e); if (q >= 108) q -= 4;
        const int hf = (q % 36) >= 18;
        const unsigned o = hf * 32 * REC + ((c0 + 1 + q / 36) & 3) * 8 * REC + (q % 18) * 1024;
        glds16<true>(p.ws, KV + o + lane * 16, c.lds0 + o);
      }
    }
  };
  const unsigned wo = OFF_WOA + m * WSL;
  auto ldw = [&](int ks) { WOUT[ks] = as_bf8(ld16w(c.rs, wo + (ft * KSTEPS + ks) * FRAG + lane * 16)); };
  auto warm_wb = [] {};
  auto dma_tail = [&](int i) { const int pc = i ? 8 : w; glds16<WSC>(p.ws, wo + 4 * KSTEPS * FRAG + pc * 1024 + lane * 16, c.lds0 + L_QST + pc * 1024); };
  STAMP(30 * l + 8);
  const f32x16 zero = {};
  const LAS char* l0 = (const LAS char*)smem;
  const LAS char* hb = l0 + kh * 32 * REC;
  const int ok0 = lane * 16, ok1 = REC_K1 + r * 4;
  const bool vreal = r < 18;
  const int ovn = REC_V + h * 288 + r * 16;
  const LAS char* vconst = l0 + L_CONST + v_const_off(r, h);
  const int vstep = vreal ? REC : 0;
  float mref[2];
#pragma unroll
  for (int a = 0; a < 2; a++) {
    const LAS char* rp = hb + c0 * 8 * REC;
    f32x16 s = MFMA32K8(mk_k1(*(const LAS unsigned*)(rp + ok1), kone), mk_q1(qt[a], 0.f, h), zero);
    s = MFMA32(ld_frag_l(rp + ok0), q0[a], s);
    mref[a] = bf_round(tile_max(s));
  }
  f32x16 oa, ob;
#define STEP(K0C, K1C, K0N, K1N, V0C, V1C, V0P, V1P, PN, PVN, HOOK) do { \
      SBAR(); \
      sb = MFMA32K8(K1C, q1b, zero); MB();                                      X4(sa, 0);  C2(tb, 0, pb1, 2); PIN(pb1); PIN(sa); SBAR(); \
      sb = MFMA32(K0C, q0[1], sb); MB(); K0N = ld_frag_l((PN) + ok0); K1N = mk_k1(*(const LAS unsigned*)((PN) + ok1), kone); MB(); X4(sa, 4);  C2(sa, 0, pa0, 0); PIN(pa0); PIN(sa); SBAR(); \
      ob = MFMA32(V0P, as_bf8(pb0), ob); MB();                                  X4(sa, 8);  C2(sa, 4, pa0, 2); PIN(pa0); PIN(sa); SBAR(); \
      ob = MFMA32(V1P, as_bf8(pb1), ob); MB(); HOOK; MB();                      XT(ta, sa); C2(sa, 8, pa1, 0); PIN(pa1); PIN(ta); SBAR(); \
      sa = MFMA32K8(K1N, q1a, zero); MB(); pv = (PVN); V0P = ld_frag_l(pv); V1P = ld_frag_l(pv + 576); MB(); X4(sb, 0); C2(ta, 0, pa1, 2); PIN(pa1); PIN(sb); SBAR(); \
      sa = MFMA32(K0N, q0[0], sa); MB();                                        X4(sb, 4);  C2(sb, 0, pb0, 0); PIN(pb0); PIN(sb); SBAR(); \
      oa = MFMA32(V0C, as_bf8(pa0), oa); MB();                                  X4(sb, 8);  C2(sb, 4, pb0, 2); PIN(pb0); PIN(sb); SBAR(); \
      oa = MFMA32(V1C, as_bf8(pa1), oa); MB();                                  XT(tb, sb); C2(sb, 8, pb1, 0); PIN(pb1); PIN(tb); SBAR(); \
    } while (0)
#define CHUNK(J, H0, H1, H2, H3, H4, H5, H6, MID, H7) do { \
      const LAS char* pn = hb + ((c0 + (J) + 1) & 3) * 8 * REC;     \
      STEP(k0A, k1A, k0B, k1B, v0A, v1A, v0B, v1B, pk + 1 * REC, pv + vstep, H0); \
      STEP(k0B, k1B, k0A, k1A, v0B, v1B, v0A, v1A, pk + 2 * REC, pv + vstep, H1); \
      STEP(k0A, k1A, k0B, k1B, v0A, v1A, v0B, v1B, pk + 3 * REC, pv + vstep, H2); \
      STEP(k0B, k1B, k0A, k1A, v0B, v1B, v0A, v1A, pk + 4 * REC, pv + vstep, H3); \
      STEP(k0A, k1A, k0B, k1B, v0A, v1A, v0B, v1B, pk + 5 * REC, pv + vstep, H4); \
      STEP(k0B, k1B, k0A, k1A, v0B, v1B, v0A, v1A, pk + 6 * REC, pv + vstep, H5); \
      STEP(k0A, k1A, k0B, k1B, v0A, v1A, v0B, v1B, pk + 7 * REC, pv + vstep, H6); \
      MID; \
      STEP(k0B, k1B, k0A, k1A, v0B, v1B, v0A, v1A, pn, vreal ? pn + ovn : vconst, H7); \
      pk = pn; \
    } while (0)
#define PASS_BEGIN() \
    const bf16x4 q1a = mk_q1(qt[0], -mref[0], h), q1b = mk_q1(qt[1], -mref[1], h); \
    const LAS char* pk = hb + c0 * 8 * REC; \
    const LAS char* pv = vreal ? pk + ovn : vconst; \
    oa = zero; ob = zero; \
    bf16x8 k0A = ld_frag_l(pk + ok0); bf16x4 k1A = mk_k1(*(const LAS unsigned*)(pk + ok1), kone); \
    bf16x8 k0B, v0A = ld_frag_l(pv), v1A = ld_frag_l(pv + 576), v0B = as_bf8(ozero4()), v1B = v0B; bf16x4 k1B; \
    f32x16 sa = MFMA32K8(k1A, q1a, zero); sa = MFMA32(k0A, q0[0], sa); \
    f32x16 sb; f32x4 ta, tb = __builtin_bit_cast(f32x4, ozero4()); \
    u32x4 pa0, pa1, pb0 = ozero4(), pb1 = pb0;
#define PASS_END() \
    C2(tb, 0, pb1, 2);     \
    ob = MFMA32(v0B, as_bf8(pb0), ob); \
    ob = MFMA32(v1B, as_bf8(pb1), ob);
#define NOH (void)0
  auto set_wait = [&] {
    if (l == 0 && w == 0) { const int gl = lane >> 3, wd = lane & 7;
      const unsigned o[4] = {flag_off(c, F_SET, gl, wd), flag_off(c, F_SET, 8 + gl, wd), flag_off(c, F_SET, 16 + gl, wd), flag_off(c, F_SET, 24 + gl, wd)};
      poll<4>(c, o, true); }
  };
#define CH0_MID WAIT_VM(0); wg_barrier()
  {
    PASS_BEGIN()
    CHUNK(0, dma2(0), dma2(1), dma2(2), dma2(3), (dma2(4), set_wait()), dma2(5), dma2(6), CH0_MID, dma_tail(0));
    CHUNK(1, dma_tail(1), ldw(0), ldw(1), ldw(2), ldw(3), ldw(4), ldw(5), NOH, ldw(6));
    CHUNK(2, ldw(7), ldw(8), ldw(9), ldw(10), ldw(11), ldw(12), ldw(13), NOH, ldw(14));
    CHUNK(3, ldw(15), ldw(16), ldw(17), NOH, warm_wb(), NOH, NOH, NOH, NOH);
    PASS_END()
  }
  if (__any((h == 0) && (!(oa[10] < L_LIMIT) || !(ob[10] < L_LIMIT)))) {
    float ma = -3.0e38f, mb = -3.0e38f;
    const bf16x4 z1a = mk_q1(qt[0], 0.f, h), z1b = mk_q1(qt[1], 0.f, h);
    for (int T = 0; T < 32; T++) {
      const LAS char* rp = hb + T * REC;
      const bf16x8 k0 = ld_frag_l(rp + ok0); const bf16x4 k1 = mk_k1(*(const LAS unsigned*)(rp + ok1), kone);
      f32x16 s = MFMA32K8(k1, z1a, zero); s = MFMA32(k0, q0[0], s); ma = fmaxf(ma, tile_max(s));
      s = MFMA32K8(k1, z1b, zero); s = MFMA32(k0, q0[1], s); mb = fmaxf(mb, tile_max(s));
    }
    mref[0] = bf_round(ma); mref[1] = bf_round(mb);
    PASS_BEGIN()
    for (int i = 0; i < 4; i++) CHUNK(i, NOH, NOH, NOH, NOH, NOH, NOH, NOH, NOH, NOH);
    PASS_END()
  }
#undef STEP
#undef CHUNK
#undef PASS_BEGIN
#undef PASS_END
  STAMP(30 * l + 9);
  if (l == 0 && w < 4) {
    fill_csb(c);
    if (!WBSC && w == 3) { __builtin_amdgcn_fence(__ATOMIC_ACQUIRE, "agent"); wait_vm0(); }
  }
  combine_store<true>(c, oa, ob, mref[0], mref[1], OFF_OT + STEP_BUF * 2 * l * XT_BYTES);
  STAMP(30 * l + 11);
  publish(c, F_O + 2 * l);
  STAMP(30 * l + 12);
}

constexpr int QB_PRE = 5;
DI void dma_wb_piece(const Ctx& c, int i) {
  int pc = c.w + 8 * i; if (pc >= 14 * QB_PRE) pc -= 8;
  glds16<WBSC>(c.p.ws, OFF_WB + ((c.m * 14 + pc / QB_PRE) * KSTEPS + pc % QB_PRE) * FRAG + c.lane * 16, c.lds0 + L_W + pc * FRAG);
}
template <int WSRC, bool LAST_OK, int NEXT> DI void unit_out(const Ctx& c_in, int li, bf16x8 (&W)[KSTEPS]) {
  const Ctx c = fresh(c_in);
  const Params& p = c.p;
  const int lane = c.lane, w = c.w, r16 = c.r16, g4 = c.g4, g = c.g, m = c.m, ft = w & 3, th = w >> 2;
  if (WSRC == 0) dma_wslice(c, ((li & 1) ? OFF_WOB : OFF_WOA) + m * WSL);
  const bool ex = (w < 2) || (w >= 6);
  const int te = w < 2 ? w : w - 4;
  const unsigned XO = OFF_XT + STEP_BUF * li * XT_BYTES;
  const int tt0 = 4 * g + 2 * th, cmain = 9 * m + 2 * ft + (g4 >> 1);
  if (WSRC == 0) { wait_vm0(); wg_barrier(); }
  bf16x8 WX[KSTEPS];
  if (WSRC != 2) wmain_from_lds(c, W);
  if (ex) wtail_from_lds(c, WX, WSRC == 2 ? L_QST : L_W + 4 * KSTEPS * FRAG);
  if (w == 7) poll1(c, flag_off(c, F_O + li, g, lane & 7), lane < 8);
  u32x2 xo0, xo1, xoe;
  if (li == 0) {
    xo0 = *(const u32x2*)(p.x0 + (size_t)(tt0 * 16 + r16) * DM + 72 * m + 16 * ft + 4 * g4);
    xo1 = *(const u32x2*)(p.x0 + (size_t)((tt0 + 1) * 16 + r16) * DM + 72 * m + 16 * ft + 4 * g4);
    xoe = *(const u32x2*)(p.x0 + (size_t)((4 * g + (ex ? te : 2 * th)) * 16 + r16) * DM + 72 * m + 64 + 4 * (g4 & 1));
  } else {
    xo0 = ld8c(c.rs, XO + ((tt0 * 72 + cmain) * 16 + r16) * 16 + (g4 & 1) * 8);
    xo1 = ld8c(c.rs, XO + (((tt0 + 1) * 72 + cmain) * 16 + r16) * 16 + (g4 & 1) * 8);
    xoe = ld8c(c.rs, XO + (((4 * g + (ex ? te : 2 * th)) * 72 + 9 * m + 8) * 16 + r16) * 16 + (g4 & 1) * 8);
  }
  wg_barrier();
  STAMP(30 * (li >> 1) + 13 * ((li & 1) ? 2 : 1) + 0);
  PIN(xo0); PIN(xo1); PIN(xoe);
  if (WSRC == 2) {
#pragma unroll
    for (int ks = 0; ks < KSTEPS; ks++) PIN(W[ks]);
  }
  dma_tile<false>(c, OFF_OT + STEP_BUF * li * XT_BYTES + g * TILE_BYTES);
  const ATiled A = {(const LAS char*)c.smem + L_A + lane * 16};
  f32x4 accx = {}, acc0 = {}, acc1 = {};
  if (!ex) gemm2<0, true>(A, 2 * th, W, WX, acc0, acc1, accx);
  else if (te & 1) gemm2<2, true>(A, 2 * th, W, WX, acc0, acc1, accx);
  else gemm2<1, true>(A, 2 * th, W, WX, acc0, acc1, accx);
  STAMP(30 * (li >> 1) + 13 * ((li & 1) ? 2 : 1) + 2);
  const unsigned a0 = cvtpk(acc0[0] + bflo(xo0[0]), acc0[1] + bfhi(xo0[0])), a1 = cvtpk(acc0[2] + bflo(xo0[1]), acc0[3] + bfhi(xo0[1]));
  const unsigned b0 = cvtpk(acc1[0] + bflo(xo1[0]), acc1[1] + bfhi(xo1[0])), b1 = cvtpk(acc1[2] + bflo(xo1[1]), acc1[3] + bfhi(xo1[1]));
  const auto x = __builtin_amdgcn_permlane16_swap(a0, b0, false, false), y = __builtin_amdgcn_permlane16_swap(a1, b1, false, false);
  const u32x4 v = {x[0], y[0], x[1], y[1]};
  const unsigned e0 = cvtpk(accx[0] + bflo(xoe[0]), accx[1] + bfhi(xoe[0])), e1 = cvtpk(accx[2] + bflo(xoe[1]), accx[3] + bfhi(xoe[1]));
  if (!LAST_OK || li < 3) {
    const unsigned XN = OFF_XT + STEP_BUF * (li + 1) * XT_BYTES;
    LAS f32x2* part = (LAS f32x2*)((LAS char*)c.smem + L_STATS);
    { const float sa = xrow16_sum(dot2(a1, 0x3f803f80u, dot2(a0, 0x3f803f80u, 0.f))), qa = xrow16_sum(dot2(a1, a1, dot2(a0, a0, 0.f)));
      const float sb = xrow16_sum(dot2(b1, 0x3f803f80u, dot2(b0, 0x3f803f80u, 0.f))), qb = xrow16_sum(dot2(b1, b1, dot2(b0, b0, 0.f)));
      if (g4 == 0) { part[ft * 64 + 32 * th + r16] = f32x2{sa, qa}; part[ft * 64 + 32 * th + 16 + r16] = f32x2{sb, qb}; }
      if (ex) { const unsigned z0 = g4 < 2 ? e0 : 0u, z1 = g4 < 2 ? e1 : 0u;
        const float se = xrow16_sum(dot2(z1, 0x3f803f80u, dot2(z0, 0x3f803f80u, 0.f))), qe = xrow16_sum(dot2(z1, z1, dot2(z0, z0, 0.f)));
        if (g4 == 0) part[4 * 64 + 16 * te + r16] = f32x2{se, qe}; } }
    st16c(c.rs, XN + (((tt0 + (g4 & 1)) * 72 + cmain) * 16 + r16) * 16, v);
    if (ex && g4 < 2) st8c(c.rs, XN + (((4 * g + te) * 72 + 9 * m + 8) * 16 + r16) * 16 + g4 * 8, u32x2{e0, e1});
    wg_barrier();
    if (w == 0) { f32x2 t = part[lane];
#pragma unroll
      for (int k = 1; k < 5; k++) t += part[k * 64 + lane];
      st8c(c.rs, OFF_XST + ((g * 8 + m) * 64 + lane) * 8, u32x2{__float_as_uint(t[0]), __float_as_uint(t[1])}); }
    if (NEXT == 1) { dma_wslice(c, OFF_WQA + m * WSL); WAIT_VM(11); }
    else if (NEXT == 2) {
#pragma unroll
      for (int i = 0; i < 9; i++) dma_wb_piece(c, i);
      WAIT_VM(9); }
    else wait_vm0();
    wg_barrier(); if (w == 0 && lane == 0) flag_set(c, F_X + li, m);
    STAMP(30 * (li >> 1) + 13 * ((li & 1) ? 2 : 1) + 3);
  } else {
    *(u32x4*)(p.out + (size_t)((tt0 + (g4 & 1)) * 16 + r16) * DM + 72 * m + 16 * ft + 8 * (g4 >> 1)) = v;
    if (ex && g4 < 2) *(u32x2*)(p.out + (size_t)((4 * g + te) * 16 + r16) * DM + 72 * m + 64 + 4 * g4) = u32x2{e0, e1};
  }
}

constexpr int QB_LA = 7;
template <bool T0, bool T1, int H1> DI void qb_loop(const Ctx& c, unsigned wo0, unsigned wo1, bf16x8 (&W0)[KSTEPS], bf16x8 (&W1)[KSTEPS], f32x4 (&acc)[2][4]) {
  const LAS char* A = (const LAS char*)c.smem + L_A + c.lane * 16;
  bf16x8 x[KSTEPS][4];
#pragma unroll
  for (int t = 0; t < 4; t++) x[0][t] = ld_frag_l(A + (t * KSTEPS + 0) * FRAG);
#pragma unroll
  for (int t = 0; t < 4; t++) x[1][t] = ld_frag_l(A + (t * KSTEPS + 1) * FRAG);
  SBAR();
#pragma unroll
  for (int ks = 0; ks < KSTEPS; ks++) {
    if (ks + QB_LA < KSTEPS) { W0[ks + QB_LA] = as_bf8(ld16b(c.rs, wo0 + (ks + QB_LA) * FRAG)); W1[ks + QB_LA] = as_bf8(ld16b(c.rs, wo1 + (ks + QB_LA) * FRAG)); }
    if (ks + 2 < KSTEPS) {
#pragma unroll
      for (int t = 0; t < 4; t++) x[ks + 2][t] = ld_frag_l(A + (t * KSTEPS + ks + 2) * FRAG);
    }
#pragma unroll
    for (int t = 0; t < 4; t++) acc[0][t] = T0 ? MFMA16x16(x[ks][t], W0[ks], acc[0][t]) : MFMA16x16(W0[ks], x[ks][t], acc[0][t]);
#pragma unroll
    for (int t = 0; t < 4; t++) if (H1 == 0 || (t >> 1) == H1 - 1) acc[1][t] = T1 ? MFMA16x16(x[ks][t], W1[ks], acc[1][t]) : MFMA16x16(W1[ks], x[ks][t], acc[1][t]);
    SBAR();
  }
}
template <bool WPRE> DI void unit_b(const Ctx& c_in, int l) {
  const Ctx c = fresh(c_in);
  const Params& p = c.p;
  const int lane = c.lane, w = c.w, r16 = c.r16, g4 = c.g4, g = c.g, m = c.m;
  char* smem = c.smem;
  const int t0 = w < 4 ? 2 * w : (w == 4 ? 9 : (w == 5 ? 11 : (w == 6 ? 8 : 13)));
  const int t1 = w < 4 ? 2 * w + 1 : ((w & 1) ? 12 : 10);
  bf16x8 W0[KSTEPS], W1[KSTEPS];
  const unsigned wo0 = OFF_WB + (m * 14 + t0) * KSTEPS * FRAG + lane * 16, wo1 = OFF_WB + (m * 14 + t1) * KSTEPS * FRAG + lane * 16;
  if (!WPRE) {
#pragma unroll
    for (int i = 0; i < 9; i++) dma_wb_piece(c, i);
    if (w < 4) fill_csb(c);
    wait_vm0(); wg_barrier();
  }
  if (w == 7) poll1(c, flag_off(c, F_X + 2 * l, g, lane & 7), lane < 8);
  wait_vm0();
  wg_barrier();
  { const LAS char* wl = (const LAS char*)smem + L_W + lane * 16;
#pragma unroll
    for (int ks = 0; ks < QB_PRE; ks++) { W0[ks] = ld_frag_l(wl + (t0 * QB_PRE + ks) * FRAG); W1[ks] = ld_frag_l(wl + (t1 * QB_PRE + ks) * FRAG); } }
  STAMP(30 * l + 17);
  XStat xst; if (w == 6) xst = xstat_load(c);
  dma_tile<false>(c, OFF_XT + STEP_BUF * (2 * l + 1) * XT_BYTES + g * TILE_BYTES);
#pragma unroll
  for (int ks = QB_PRE; ks < QB_LA; ks++) { W0[ks] = as_bf8(ld16b(c.rs, wo0 + ks * FRAG)); W1[ks] = as_bf8(ld16b(c.rs, wo1 + ks * FRAG)); }
  WAIT_VM(4);
  if (w == 6) xstat_finish(c, xst);
  wg_barrier();
  f32x4 acc[2][4] = {};
  if (w < 4) qb_loop<false, false, 0>(c, wo0, wo1, W0, W1, acc);
  else if (w < 6) qb_loop<true, true, 1>(c, wo0, wo1, W0, W1, acc);
  else if (w == 6) qb_loop<false, true, 2>(c, wo0, wo1, W0, W1, acc);
  else qb_loop<true, true, 2>(c, wo0, wo1, W0, W1, acc);
  STAMP(30 * l + 19);
  wg_barrier();
  STAMP(30 * l + 20);
  const LAS float* CS = (const LAS float*)((const LAS char*)smem + L_CSB);
  const unsigned KV = OFF_KVB + STEP_BUF * l * KVB_BYTES;
  LAS char* qs = (LAS char*)smem + L_QSTB;
  const float qsc = __uint_as_float(oconst<0x3eae1a92u>());
  if (w < 4 || w == 6) {
    f32x2 st[4];
#pragma unroll
    for (int t = 0; t < 4; t++) st[t] = tok_stat(c, 16 * t + r16);
    if (w < 4) {
#pragma unroll
      for (int i = 0; i < 2; i++) {
        const int tile = t0 + i; const bool isq = tile < 4; const int hl = tile & 3;
        const f32x4 cs = *(const LAS f32x4*)(CS + tile * 16 + 4 * g4); const float sc = isq ? qsc : 1.0f;
#pragma unroll
        for (int u = 0; u < 2; u++) {
          const f32x4 c0 = acc[i][2 * u], c1 = acc[i][2 * u + 1];
          const float m0 = st[2 * u][0], m1 = st[2 * u + 1][0], r0 = st[2 * u][1] * sc, r1 = st[2 * u + 1][1] * sc;
          const unsigned a0 = cvtpk((c0[0] - m0 * cs[0]) * r0, (c0[1] - m0 * cs[1]) * r0), a1 = cvtpk((c0[2] - m0 * cs[2]) * r0, (c0[3] - m0 * cs[3]) * r0);
          const unsigned b0 = cvtpk((c1[0] - m1 * cs[0]) * r1, (c1[1] - m1 * cs[1]) * r1), b1 = cvtpk((c1[2] - m1 * cs[2]) * r1, (c1[3] - m1 * cs[3]) * r1);
          const auto x = __builtin_amdgcn_permlane16_swap(a0, b0, false, false), y = __builtin_amdgcn_permlane16_swap(a1, b1, false, false);
          const u32x4 v = {x[0], y[0], x[1], y[1]};
          const int tl = 16 * (g4 & 1) + r16;
          if (isq) *(LAS u32x4*)(qs + (hl * 64 + 32 * u + tl) * 32 + (g4 >> 1) * 16) = v;
          else st16c(c.rs, KV + ((4 * m + hl) * 64 + 2 * g + u) * REC + ((g4 >> 1) * 32 + tl) * 16, v);
        }
      }
    } else {
      const f32x4 cs0 = *(const LAS f32x4*)(CS + 8 * 16 + 4 * g4);
      const bool isq = g4 < 2; const float sc = isq ? qsc : 1.0f; const int hp = 2 * (g4 & 1);
#pragma unroll
      for (int t = 0; t < 4; t++) {
        const f32x4 cc = acc[0][t]; const float mm = st[t][0], rr = st[t][1] * sc;
        const unsigned a0 = cvtpk((cc[0] - mm * cs0[0]) * rr, (cc[1] - mm * cs0[1]) * rr), a1 = cvtpk((cc[2] - mm * cs0[2]) * rr, (cc[3] - mm * cs0[3]) * rr);
        if (isq) {
          *(LAS unsigned*)(qs + 8192 + (hp * 64 + 16 * t + r16) * 4) = a0;
          *(LAS unsigned*)(qs + 8192 + ((hp + 1) * 64 + 16 * t + r16) * 4) = a1;
        } else {
          const int T = 2 * g + (t >> 1), key = 16 * (t & 1) + r16;
          st4c(c.rs, KV + ((4 * m + hp) * 64 + T) * REC + REC_K1 + key * 4, a0);
          st4c(c.rs, KV + ((4 * m + hp + 1) * 64 + T) * REC + REC_K1 + key * 4, a1);
        }
      }
    }
  }
  if (w >= 4) {
    auto vout = [&](int tile, int u, const f32x4& a0, const f32x4& a1) {
      const float cs = CS[tile * 16 + r16];
      unsigned pk[2][2];
#pragma unroll
      for (int tt = 0; tt < 2; tt++) {
        const LAS f32x4* sp = (const LAS f32x4*)((const LAS char*)smem + L_STAB + (16 * (2 * u + tt) + 4 * g4) * 8);
        const f32x4 sA = sp[0], sB = sp[1], a = tt ? a1 : a0;
        pk[tt][0] = cvtpk((a[0] - sA[0] * cs) * sA[1], (a[1] - sA[2] * cs) * sA[3]);
        pk[tt][1] = cvtpk((a[2] - sB[0] * cs) * sB[1], (a[3] - sB[2] * cs) * sB[3]);
      }
      const auto x = __builtin_amdgcn_permlane32_swap(pk[0][0], pk[1][0], false, false), y = __builtin_amdgcn_permlane32_swap(pk[0][1], pk[1][1], false, false);
      const u32x4 v = {x[0], y[0], x[1], y[1]};
      if (tile < 13) st16c(c.rs, KV + ((4 * m + tile - 9) * 64 + 2 * g + u) * REC + REC_V + (g4 * 18 + r16) * 16, v);
      else if (r16 < 8) st16c(c.rs, KV + ((4 * m + (r16 >> 1)) * 64 + 2 * g + u) * REC + REC_V + (g4 * 18 + 16 + (r16 & 1)) * 16, v);
    };
    if (w != 6) { vout(t0, 0, acc[0][0], acc[0][1]); vout(t0, 1, acc[0][2], acc[0][3]); }
    if (w < 6) vout(t1, 0, acc[1][0], acc[1][1]); else vout(t1, 1, acc[1][2], acc[1][3]);
  }
  publish(c, F_KB + l);
  STAMP(30 * l + 21);
  const int r = lane & 31, h = lane >> 5, hl = w & 3, kh = w >> 2;
  const int li = 2 * l + 1, head = 4 * m + hl;
  const unsigned KH = KV + head * 64 * REC;
  const unsigned ok0 = lane * 16, ok1 = REC_K1 + r * 4;
  const bool vreal = r < 18;
  const unsigned ovn = REC_V + h * 288 + r * 16;
  const int Tr = 2 * g - 9 + 4 * kh;
  auto tile_of = [&](int j) { return j == 0 ? 2 * g + kh : Tr + j; };
  const unsigned kone = oconst<0x3f80u>();
  bf16x8 K0[5], V0[5], V1[5], KS0; unsigned K1[5], KS1;
  const unsigned vp1 = r == 18 ? oconst<0x3f803f80u>() : ozero(); const u32x4 vpad = {vp1, vp1, vp1, vp1};
  auto ldk = [&](int j) { const int T = tile_of(j); const unsigned rp = KH + (T < 0 ? 0 : T) * REC;
    K0[j] = as_bf8(ld16c(c.rs, rp + ok0)); K1[j] = ld4c(c.rs, rp + ok1); };
  auto ldv = [&](int j) { const int T = tile_of(j); const unsigned rp = KH + (T < 0 ? 0 : T) * REC;
    u32x4 a = vpad, b = vpad;
    if (vreal) { a = ld16c(c.rs, rp + ovn); b = ld16c(c.rs, rp + ovn + 576); }
    V0[j] = as_bf8(a); V1[j] = as_bf8(b); };
  ldk(0);
  { const unsigned rp = KH + 2 * g * REC; KS0 = as_bf8(ld16c(c.rs, rp + ok0)); KS1 = ld4c(c.rs, rp + ok1); }
  ldv(0);
  auto wpiece = [&](int i) {
    if (w < 4) { int fr = w + 4 * i; if (fr > 80) fr = 80; glds16<WSC>(p.ws, OFF_WOB + m * WSL + fr * FRAG + lane * 16, c.lds0 + L_W + fr * FRAG); } };
  auto wp4 = [&](int i) { wpiece(i); wpiece(i + 1); wpiece(i + 2); wpiece(i + 3); };
  bf16x8 q0[2]; unsigned qt[2];
#pragma unroll
  for (int a = 0; a < 2; a++) q_from_stage(c, L_QSTB, hl, 32 * a + r, q0[a], qt[a]);
  const f32x16 zero = {};
  f32x16 md, md1, mf; const float NEG = __uint_as_float(oconst<0xf149f2cau>());
#pragma unroll
  for (int i = 0; i < 16; i++) { const int cc = (i & 3) + 8 * (i >> 2) + 4 * h;
    md[i] = cc > r ? NEG : 0.f; md1[i] = (kh == 1 && cc > r) ? NEG : 0.f; mf[i] = (kh == 0 && cc <= r) ? NEG : 0.f; }
  auto valid = [&](int a, int j) { return tile_of(j) >= 0 && !(kh == 0 ? (a == 1 && j == 1) : (a == 0 && j == 0)); };
#define B_MASK(a, j) ((a) == 0 && (j) == 0 ? md : ((a) == 1 && (j) == 0 ? md1 : ((((a) == 0 && (j) == 1) || ((a) == 1 && (j) == 2)) ? mf : zero)))
#define B_QK(a, j, Q1V, Q1OFF) MFMA32(K0[j], q0[a], MFMA32K8(mk_k1(K1[j], kone), valid(a, j) ? (Q1V) : (Q1OFF), B_MASK(a, j)))
  const bf16x4 offa = mk_q1(qt[0], -1e30f, h), offb = mk_q1(qt[1], -1e30f, h);
  float mref[2];
  { const f32x16 s = MFMA32(KS0, q0[0], MFMA32K8(mk_k1(KS1, kone), mk_q1(qt[0], 0.f, h), md)); mref[0] = bf_round(tile_max(s)); }
  { const f32x16 s = B_QK(1, 0, mk_q1(qt[1], 0.f, h), offb); mref[1] = bf_round(tile_max(s)); }
  STAMP(30 * l + 23);
  if (w == 3) { const int gg = g - (lane & 7); poll1(c, flag_off(c, F_KB + l, gg < 0 ? 0 : gg, m), lane < 5 && gg >= 0); }
  wg_barrier();
  STAMP(30 * l + 22);
  ldk(1); ldk(2); ldv(1);
  f32x16 oa, ob;
#define BSTEP(j, H1, H2) do { \
      SBAR(); \
      sb = MFMA32K8(mk_k1(K1[j], kone), valid(1, j) ? q1b : offb, B_MASK(1, j)); MB(); H1; MB();    X4(sa, 0);  C2(tb, 0, pb1, 2); PIN(pb1); PIN(sa); SBAR(); \
      sb = MFMA32(K0[j], q0[1], sb); MB();                                                X4(sa, 4);  C2(sa, 0, pa0, 0); PIN(pa0); PIN(sa); SBAR(); \
      if ((j) > 0) ob = MFMA32(V0[(j) > 0 ? (j) - 1 : 0], as_bf8(pb0), ob); MB(); H2; MB();   X4(sa, 8);  C2(sa, 4, pa0, 2); PIN(pa0); PIN(sa); SBAR(); \
      if ((j) > 0) ob = MFMA32(V1[(j) > 0 ? (j) - 1 : 0], as_bf8(pb1), ob); MB();         XT(ta, sa); C2(sa, 8, pa1, 0); PIN(pa1); PIN(ta); SBAR(); \
      if ((j) < 4) sa = MFMA32K8(mk_k1(K1[(j) < 4 ? (j) + 1 : 0], kone), valid(0, (j) + 1) ? q1a : offa, B_MASK(0, (j) + 1)); MB(); X4(sb, 0); C2(ta, 0, pa1, 2); PIN(pa1); PIN(sb); SBAR(); \
      if ((j) < 4) sa = MFMA32(K0[(j) < 4 ? (j) + 1 : 0], q0[0], sa); MB();               X4(sb, 4);  C2(sb, 0, pb0, 0); PIN(pb0); PIN(sb); SBAR(); \
      oa = MFMA32(V0[j], as_bf8(pa0), oa); MB();                                          X4(sb, 8);  C2(sb, 4, pb0, 2); PIN(pb0); PIN(sb); SBAR(); \
      oa = MFMA32(V1[j], as_bf8(pa1), oa); MB();                                          XT(tb, sb); C2(sb, 8, pb1, 0); PIN(pb1); PIN(tb); SBAR(); \
    } while (0)
#define BPASS(A1, A2, B1, B2, C1, C2_, D1, D2, E1, E2) do { \
      const bf16x4 q1a = mk_q1(qt[0], -mref[0], h), q1b = mk_q1(qt[1], -mref[1], h); \
      oa = zero; ob = zero; \
      f32x16 sa = B_QK(0, 0, q1a, offa), sb; f32x4 ta, tb = __builtin_bit_cast(f32x4, ozero4()); \
      u32x4 pa0, pa1, pb0, pb1 = ozero4(); \
      BSTEP(0, A1, A2); BSTEP(1, B1, B2); BSTEP(2, C1, C2_); BSTEP(3, D1, D2); BSTEP(4, E1, E2); \
      C2(tb, 0, pb1, 2); \
      ob = MFMA32(V0[4], as_bf8(pb0), ob); \
      ob = MFMA32(V1[4], as_bf8(pb1), ob); \
    } while (0)
#define NOH (void)0
  BPASS(ldk(3), ldv(2), ldk(4), ldv(3), ldv(4), wp4(0), wp4(4), wp4(8), wp4(12), (wp4(16), wpiece(20)));
  if (__any((h == 0) && (!(oa[10] < L_LIMIT) || !(ob[10] < L_LIMIT)))) {
    float ma = -3.0e38f, mb = -3.0e38f;
    const bf16x4 za = mk_q1(qt[0], 0.f, h), zb = mk_q1(qt[1], 0.f, h);
#define B_MX(j) do { const f32x16 s0 = B_QK(0, j, za, offa), s1 = B_QK(1, j, zb, offb); ma = fmaxf(ma, tile_max(s0)); mb = fmaxf(mb, tile_max(s1)); } while (0)
    B_MX(0); B_MX(1); B_MX(2); B_MX(3); B_MX(4);
#undef B_MX
    mref[0] = bf_round(ma); mref[1] = bf_round(mb);
    BPASS(NOH, NOH, NOH, NOH, NOH, NOH, NOH, NOH, NOH, NOH);
  }
#undef BPASS
#undef BSTEP
#undef NOH
#undef B_QK
#undef B_MASK
  STAMP(30 * l + 24);
  combine_store<false>(c, oa, ob, mref[0], mref[1], OFF_OT + STEP_BUF * li * XT_BYTES);
  publish(c, F_O + li);
  STAMP(30 * l + 25);
}

__global__ void __launch_bounds__(512, 2) k_main(Params p, int ub, int ue) {
  extern __shared__ __attribute__((aligned(16))) char smem[];
  Ctx c;
  c.p = p; c.smem = smem; c.lds0 = (unsigned)(uintptr_t)smem;
  c.rs = __builtin_amdgcn_make_buffer_rsrc(p.ws, 0, WS_SPAN, 0x00020000);
  { const int b = blockIdx.x, xcd = b & 7, idx = b >> 3; c.b = b; c.g = xcd * 4 + (idx >> 3); c.m = idx & 7; }
  c.lane = threadIdx.x & 63; c.w = __builtin_amdgcn_readfirstlane(threadIdx.x >> 6);
  c.r16 = c.lane & 15; c.g4 = c.lane >> 4;
  c.tag = *(const unsigned long long*)(p.ws + OFF_EPOCH) + TAG_STEP;
  {
    const char* pc; asm volatile("s_getpc_b64 %0" : "=s"(pc));
    const Ctx e = fresh(c);
    if (e.w == 7) { const unsigned o = ((e.b >> 3) * 64 + e.lane) * 128; glds4(pc, o < CODE_PREFETCH ? o : 0u, e.lds0 + L_JUNK); }
  }
  STAMP(0);
  for (int l = 0; l < 2; l++) {
    const int u = 4 * l;
    bf16x8 W[KSTEPS];
    if (l == 0) unit_qa<true, false>(c, l); else unit_qa<false, true>(c, l);
    unit_attn_a(c, l, W);
    unit_out<2, false, 2>(c, 2 * l, W);
    unit_b<true>(c, l);
    unit_out<1, true, 1>(c, 2 * l + 1, W);
    STAMP(30 * l + 30);
  }
  { const Ctx e = fresh(c); if (ue == 8 && e.b == 0 && e.w == 0 && e.lane == 0) *(unsigned long long*)(p.ws + OFF_EPOCH) = e.tag; }
}

extern "C" void kernel_launch(void* const* d_in, const int* in_sizes, int n_in, void* d_out, int out_size, void* d_ws, size_t ws_size,
                              hipStream_t stream) {
  Params p;
  p.x0 = (const unsigned short*)d_in[0]; p.wq = (const unsigned short*)d_in[1]; p.wk = (const unsigned short*)d_in[2];
  p.wv = (const unsigned short*)d_in[3]; p.woA = (const unsigned short*)d_in[4];
  p.wqkv = (const unsigned short*)d_in[6]; p.woB = (const unsigned short*)d_in[7];
  p.ws = (char*)d_ws;
  p.out = (unsigned short*)d_out;
  (void)hipFuncSetAttribute((const void*)k_main, hipFuncAttributeMaxDynamicSharedMemorySize, LDS_TOTAL);
  k_main<<<256, 512, LDS_TOTAL, stream>>>(p, 0, 8);
}
```
